```python
import torch

def _softmax(x, axis=-1):
    return torch.softmax(x, dim=axis)
def _gelu(x):
    return torch.nn.functional.gelu(x, approximate="tanh")
def _silu(x):
    return torch.nn.functional.silu(x)
def _sigmoid(x):
    return torch.sigmoid(x)
def _layernorm(x, axis=-1, eps=1e-5):
    m = x.mean(dim=axis, keepdim=True)
    v = x.var(dim=axis, keepdim=True, unbiased=False)
    return (x - m) / torch.sqrt(v + eps)
def _scatter_add(tgt, idx, src, axis=0):
    return tgt.clone().index_add_(axis, idx, src)
def _segment_sum(src, seg, n):
    out = torch.zeros((n,) + src.shape[1:], dtype=src.dtype)
    return out.index_add_(0, seg, src)
def _pad(x, pw):
    flat = tuple(v for pair in reversed(pw) for v in pair)
    return torch.nn.functional.pad(x, flat)

kernel_name = 'gated_mix__mha_block__geglu_ffn__prompt'

def setup_inputs():

    _w = (lambda a: (lambda t: t.to(torch.bfloat16) if t.is_floating_point() else t)(torch.as_tensor(a)))
    def _A_setup():
        B, S, D, H = 1, 2048, 512, 32
        return {
            "x": _w((lambda *s: torch.randn(*s, dtype=torch.bfloat16) * 0.05)(B, S, D)),
            "wqkv": _w((lambda *s: torch.randn(*s, dtype=torch.bfloat16) * 0.8)(D, 3 * D)),
            "wo": _w((lambda *s: torch.randn(*s, dtype=torch.bfloat16) * 0.05)(D, D)),
        }
    _A_in = _A_setup()

    def _B_setup():
        B, S, D, F = 1, 2048, 512, 1376
        return {
            "x": _w((lambda *s: torch.randn(*s, dtype=torch.bfloat16) * 0.05)(B, S, D)),
            "w_gate": _w((lambda *s: torch.randn(*s, dtype=torch.bfloat16) * 0.1)(D, F)),
            "w_up": _w((lambda *s: torch.randn(*s, dtype=torch.bfloat16) * 0.1)(D, F)),
            "w_down": _w((lambda *s: torch.randn(*s, dtype=torch.bfloat16) * 0.1)(F, D)),
        }
    _B_in = _B_setup()

    return {
        "A_x": _A_in["x"],
        "A_wqkv": _A_in["wqkv"],
        "A_wo": _A_in["wo"],
        "B_x": _B_in["x"],
        "B_w_gate": _B_in["w_gate"],
        "B_w_up": _B_in["w_up"],
        "B_w_down": _B_in["w_down"],
        "gate_w": _w((lambda *s: torch.randn(*s, dtype=torch.bfloat16) * 0.05)(512) * 16.0),
    }

def reference(A_x, A_wqkv, A_wo, B_x, B_w_gate, B_w_up, B_w_down, gate_w):
    def _A_ref(x, wqkv, wo):
        B, S, D = x.shape
        H = 32
        Dh = D // H
        qkv = (lambda x, *d: x.permute(*d))((x @ wqkv).reshape(B, S, 3, H, Dh), 2, 0, 3, 1, 4)
        q, k, v = qkv[0], qkv[1], qkv[2]
        scores = torch.einsum("bhqd,bhkd->bhqk", q, k) * (Dh ** -0.5)
        p = _softmax(scores, axis=-1)
        o = torch.einsum("bhqk,bhkd->bhqd", p, v)
        return (lambda x, *d: x.permute(*d))(o, 0, 2, 1, 3).reshape(B, S, D) @ wo

    def _B_ref(x, w_gate, w_up, w_down):
        return (_gelu(x @ w_gate) * (x @ w_up)) @ w_down

    x = A_x
    g = _sigmoid((x * gate_w).sum(-1, keepdims=True))
    a = _A_ref(x, A_wqkv, A_wo)
    b = _B_ref(x, B_w_gate, B_w_up, B_w_down)
    return g * a + (1 - g) * b
```

```cpp
#include <hip/hip_runtime.h>
#include <hip/hip_bf16.h>

typedef float f32x2 __attribute__((ext_vector_type(2)));
typedef float f32x4 __attribute__((ext_vector_type(4)));
typedef float f32x8 __attribute__((ext_vector_type(8)));
typedef float f32x16 __attribute__((ext_vector_type(16)));
typedef __bf16 bf16x2 __attribute__((ext_vector_type(2)));
typedef __bf16 bf16x8 __attribute__((ext_vector_type(8)));
typedef __bf16 bf16x16 __attribute__((ext_vector_type(16)));
typedef short s16x4 __attribute__((ext_vector_type(4)));
typedef unsigned u32x2 __attribute__((ext_vector_type(2)));
typedef unsigned u32x4 __attribute__((ext_vector_type(4)));
#define LAS __attribute__((address_space(3)))
#define DI __device__ __forceinline__

constexpr int SEQ = 2048, DM = 512, FF = 1376;
constexpr int NKS = 59;
constexpr unsigned WS_Q = 0, WS_K = 2u << 20, WS_V = 4u << 20, WS_A = 8u << 20, WS_G = 16u << 20, WS_END = 17u << 20;
constexpr int WPART = 16 * 784;
constexpr int SLOT = 16384 + WPART, LDS_XCH = 5 * SLOT, LDS_GW = LDS_XCH + 8 * 512, LDS_BYTES = 160 * 1024;
constexpr float QSCALE = 0.25f * 1.4426950408889634f;

DI u32x4 make_srd(const void* p, unsigned bytes) {
    const unsigned long long a = (unsigned long long)p;
    return (u32x4){(unsigned)a, (unsigned)(a >> 32) & 0xffffu, bytes, 0x00020000u};
}
DI void dma16(u32x4 srd, unsigned voff, unsigned soff, unsigned lds) {
    asm volatile("s_mov_b32 m0, %0\n\ts_nop 0\n\tbuffer_load_dwordx4 %1, %2, %3 offen lds" ::"s"(lds), "v"(voff), "s"(srd), "s"(soff) : "memory");
}
DI void dma16_masked(u32x4 srd, unsigned voff, unsigned soff, unsigned lds, unsigned long long mask) {
    asm volatile("s_mov_b32 m0, %0\n\ts_mov_b64 exec, %4\n\tbuffer_load_dwordx4 %1, %2, %3 offen lds\n\ts_mov_b64 exec, -1"
                 ::"s"(lds), "v"(voff), "s"(srd), "s"(soff), "s"(mask) : "memory");
}
template <int N> DI void vm_bar() { asm volatile("s_waitcnt vmcnt(%0)\n\ts_barrier" ::"i"(N) : "memory"); }
#define SCHED() __builtin_amdgcn_sched_barrier(0)
template <int I> struct IC { static constexpr int v = I; };
template <int B, int E, class Fn> DI void static_for(Fn&& f) { if constexpr (B < E) { f(IC<B>{}); static_for<B + 1, E>(f); } }
DI void dma16x2_masked(u32x4 srd, unsigned voff, unsigned soff0, unsigned lds0, unsigned soff1, unsigned lds1, unsigned long long mask) {
    asm volatile("s_mov_b32 m0, %0\n\ts_mov_b64 exec, %6\n\tbuffer_load_dwordx4 %1, %2, %3 offen lds\n\t"
                 "s_mov_b32 m0, %4\n\ts_nop 0\n\tbuffer_load_dwordx4 %1, %2, %5 offen lds\n\ts_mov_b64 exec, -1"
                 ::"s"(lds0), "v"(voff), "s"(srd), "s"(soff0), "s"(lds1), "s"(soff1), "s"(mask) : "memory");
}
#define WAIT_BAR(N) asm volatile("s_waitcnt vmcnt(" #N ") lgkmcnt(0)\n\ts_barrier" ::: "memory")
#define LGKM_BAR() asm volatile("s_waitcnt lgkmcnt(0)\n\ts_barrier" ::: "memory")
template <int N> DI void wait_bar() { asm volatile("s_waitcnt vmcnt(%0) lgkmcnt(0)\n\ts_barrier" ::"i"(N) : "memory"); }

#if __HIP_DEVICE_COMPILE__
#define MFMA(a, b, c) __builtin_amdgcn_mfma_f32_32x32x16_bf16((a), (b), (c), 0, 0, 0)
#define SMFMAC(a, b, c) __builtin_amdgcn_smfmac_f32_32x32x32_bf16((a), __builtin_bit_cast(bf16x16, (b)), (c), (int)0xDDDDDDDD, 0, 0)
#define MFMA16(a, b, c) __builtin_amdgcn_mfma_f32_16x16x32_bf16((a), (b), (c), 0, 0, 0)
#define TRREAD(a) __builtin_amdgcn_ds_read_tr16_b64_v4i16((LAS s16x4*)(size_t)(a))
#else
#define MFMA(a, b, c) (c)
#define SMFMAC(a, b, c) (c)
#define MFMA16(a, b, c) (c)
#define TRREAD(a) (s16x4{})
#endif
DI bf16x8 lds_frag(unsigned a) { return *(const LAS bf16x8*)(size_t)a; }
DI bf16x8 lds_frag_tr(unsigned a, int step) {
#if __HIP_DEVICE_COMPILE__
    const s16x4 lo = __builtin_amdgcn_ds_read_tr16_b64_v4i16((LAS s16x4*)(size_t)a);
    const s16x4 hi = __builtin_amdgcn_ds_read_tr16_b64_v4i16((LAS s16x4*)(size_t)(a + step));
    return __builtin_bit_cast(bf16x8, __builtin_shufflevector(lo, hi, 0, 1, 2, 3, 4, 5, 6, 7));
#else
    return bf16x8{};
#endif
}
DI unsigned pk2(float a, float b) { return __builtin_bit_cast(unsigned, __builtin_convertvector((f32x2){a, b}, bf16x2)); }
DI bf16x8 cat8(s16x4 lo, s16x4 hi) { return __builtin_bit_cast(bf16x8, __builtin_shufflevector(lo, hi, 0, 1, 2, 3, 4, 5, 6, 7)); }
DI u32x4 join16(f32x4 a, f32x4 b) {
    auto r0 = __builtin_amdgcn_permlane16_swap(pk2(a[0], a[1]), pk2(b[0], b[1]), false, false);
    auto r1 = __builtin_amdgcn_permlane16_swap(pk2(a[2], a[3]), pk2(b[2], b[3]), false, false);
    return (u32x4){r0[0], r1[0], r0[1], r1[1]};
}
DI float half_sum(float v) {
    auto r = __builtin_amdgcn_permlane32_swap(__float_as_uint(v), __float_as_uint(v), false, false);
    return __uint_as_float(r[0]) + __uint_as_float(r[1]);
}
DI float half_max(float v) {
    auto r = __builtin_amdgcn_permlane32_swap(__float_as_uint(v), __float_as_uint(v), false, false);
    return fmaxf(__uint_as_float(r[0]), __uint_as_float(r[1]));
}
typedef __amdgpu_buffer_rsrc_t rsrc_t;
DI rsrc_t make_rsrc(const void* p, unsigned bytes) { return __builtin_amdgcn_make_buffer_rsrc((void*)p, 0, bytes, 0x00020000); }
constexpr int AUX_DEV = 16;
DI void st_ws(rsrc_t ws, unsigned voff, unsigned soff, u32x4 v) { __builtin_amdgcn_raw_buffer_store_b128(v, ws, voff + soff, 0, AUX_DEV); }
DI bf16x8 ld_ws(rsrc_t ws, unsigned voff, unsigned soff) { return __builtin_bit_cast(bf16x8, __builtin_amdgcn_raw_buffer_load_b128(ws, voff, soff, AUX_DEV)); }

#define STAMP(i)
#define STAMP_EVERY 1
#define STM_ARG
#define STM_PASS
struct Ctx {
    const void *x, *wqkv, *wo, *wg, *wu, *wd, *gw;
    void *out, *ws;
};

struct P1Plan {
    static constexpr int order[16] = {0, 1, 8, 2, 3, 9, 4, 5, 10, 6, 7, 11, 12, 13, 14, 15};
    static constexpr int cnt(int j) { return order[j] < 8 ? 7 : 12; }
    static constexpr int first(int j) { int n = 0; for (int i = 0; i < j; ++i) n += cnt(i); return n; }
    static constexpr int stage(int n) { int j = 0; while (j < 15 && first(j + 1) <= n) ++j; return j; }
    static constexpr int pos(int c) { int j = 0; while (order[j] != c) ++j; return j; }
    static constexpr int total = 8 * 7 + 8 * 12;
    static constexpr int PRO = 12, PACE = 2;
    static constexpr int when(int p) {
        int t = -PACE;
        for (int q = PRO; q <= p; ++q) { const int lo = q / 4 >= 5 ? first(q / 4 - 3) : 0; t = t + PACE > lo ? t + PACE : lo; }
        return t;
    }
    static constexpr int piece_at(int n) { for (int p = PRO; p < 64; ++p) if (when(p) == n) return p; return -1; }
    static constexpr int need(int j) { return j == 14 ? 15 : j; }
    static constexpr int inflight(int j) { int n = 0; for (int p = 4 * need(j) + 4; p < 64; ++p) if (p < PRO || when(p) < first(j)) ++n; return n; }
};
DI float phase1(const Ctx& c, unsigned lds0, int rb, int cg, int w, int lane STM_ARG) {
    const int mt = w & 3, nh = w >> 2, r16 = lane & 15, g4 = lane >> 4;
    const u32x4 sx = make_srd(c.x, SEQ * DM * 2), sq = make_srd(c.wqkv, DM * 3 * DM * 2);
    const u32x4 sg = make_srd(c.wg, DM * FF * 2), su = make_srd(c.wu, DM * FF * 2), sw = make_srd(c.gw, DM * 2);
    const rsrc_t ws = make_rsrc(c.ws, WS_END);
    const int ustart = cg < 6 ? 6 * cg : 5 * cg + 6, nunit = cg < 6 ? 6 : 5;
    const unsigned long long m12 = 0x0000FFFFFFFFFFFFull, mffn = cg < 6 ? m12 : 0x00003FF3FF3FF3FFull;
    const int wr = lane / 12, wc = lane - 12 * wr;
    const unsigned vx0 = (rb * 128 + 16 * w + (lane >> 3)) * 1024 + (((lane & 7) ^ (lane >> 4)) << 4);
    const unsigned vx1 = (rb * 128 + 16 * w + 8 + (lane >> 3)) * 1024 + (((lane & 7) ^ (4 + (lane >> 4))) << 4);
    const unsigned vwq = (8 * w + wr) * 3072 + (wc < 8 ? cg * 128 + wc * 16 : 2048 + cg * 64 + (wc - 8) * 16);
    const unsigned vwf = (8 * w + wr) * 2752 + ustart * 32 + wc * 16;
    auto issue = [&](int j, int pc) {
        const unsigned sl = lds0 + (j % 5) * SLOT;
        const int cs = P1Plan::order[j], i = pc & 1;
        if (cs < 8) {
            if (pc < 2) dma16(sx, i ? vx1 : vx0, cs * 128, sl + w * 2048 + i * 1024);
            else dma16_masked(sq, vwq, (cs * 64 + 4 * i) * 3072, sl + 16384 + (2 * w + i) * 784, m12);
        } else {
            dma16_masked(pc < 2 ? sg : su, vwf, ((cs - 8) * 64 + 4 * i) * 2752, sl + (pc < 2 ? 0 : WPART) + (2 * w + i) * 784, mffn);
        }
    };
    dma16(sw, lane * 16, 0, lds0 + LDS_GW);
#pragma unroll
    for (int j = 0; j < P1Plan::PRO / 4; ++j) {
#pragma unroll
        for (int pc = 0; pc < 4; ++pc) issue(j, pc);
        if (j + 1 < P1Plan::PRO / 4) __builtin_amdgcn_s_barrier();
    }
    unsigned xa[2];
#pragma unroll
    for (int ss = 0; ss < 2; ++ss) xa[ss] = lds0 + (32 * mt + r16) * 128 + (((g4 ^ (r16 >> 1) ^ (nh << 2)) ^ (4 * ss)) << 4);
    const unsigned wb = lds0 + 2 * g4 * 784 + (r16 >> 2) * 192 + 8 * (lane & 3);
    unsigned qa[2], va[2], fa[2];
#pragma unroll
    for (int ss = 0; ss < 2; ++ss) {
        const unsigned o = wb + (ss ^ nh) * 8 * 784;
        qa[ss] = o + 16384 + 64 * nh; va[ss] = o + 16384 + 128 + 32 * nh; fa[ss] = o + 96 * nh;
    }
    const unsigned gwa = lds0 + LDS_GW + 16 * g4 + 64 * nh;
    const int tile = rb * 4 + mt;

    bf16x8 xf[16][2];
    s16x4 wlo[4], whi[4];
    f32x4 aq[2][2] = {}, av[2] = {}, ag[2] = {}, af[6][2] = {};
    float z0 = 0.f, z1 = 0.f;
    unsigned bx[2], bq[2], bv[2];
    constexpr int PD = 3;
    static_for<-PD, P1Plan::total>([&](auto I) {
        constexpr int n = I.v, nn = n + PD;
        constexpr int jn = P1Plan::stage(nn < P1Plan::total ? nn : 0), kn = nn - P1Plan::first(jn), cn = P1Plan::order[jn];
        constexpr int j = P1Plan::stage(n < 0 ? 0 : n), k = n - P1Plan::first(j), cs = P1Plan::order[j];
        constexpr bool fetch = nn < P1Plan::total;
        constexpr int ssn = cn < 8 ? (kn >= 4) : kn / 6, un = cn < 8 ? (kn >= 4 ? kn - 4 : kn) : kn % 6, son = (jn % 5) * SLOT;
        constexpr int ss = cs < 8 ? (k >= 4) : k / 6, u = cs < 8 ? (k >= 4 ? k - 4 : k) : k % 6;
        if constexpr (fetch && kn == 0) {
            if constexpr (jn == 0) { STAMP(1); }
            if constexpr (jn < 15) vm_bar<P1Plan::inflight(jn)>();
            if constexpr (jn == 0 || jn % STAMP_EVERY == 0) { STAMP(2 + jn); }
#pragma unroll
            for (int q = 0; q < 2; ++q) {
                if constexpr (cn < 8) { bx[q] = xa[q] + son; bq[q] = qa[q] + son; bv[q] = va[q] + son; asm volatile("" : "+v"(bx[q]), "+v"(bq[q]), "+v"(bv[q])); }
                else { bq[q] = fa[q] + son; asm volatile("" : "+v"(bq[q])); }
            }
        }
        unsigned fad = 0;
        if constexpr (fetch) fad = cn < 8 ? (un < 2 ? bq[ssn] + 32 * un : bv[ssn]) : bq[ssn] + (un & 1) * WPART + (un >> 1) * 32;
        auto mul = [&](int m) {
            if constexpr (n >= 0) {
                const bf16x8 wv = cat8(wlo[n & 3], whi[n & 3]), xv = xf[2 * (cs & 7) + ss][m];
                if constexpr (cs >= 8) af[u][m] = MFMA16(wv, xv, af[u][m]);
                else if constexpr (u < 2) aq[u][m] = MFMA16(wv, xv, aq[u][m]);
                else if constexpr (u == 2) av[m] = MFMA16(xv, wv, av[m]);
                else ag[m] = MFMA16(wv, xv, ag[m]);
            }
        };
        mul(0);
        SCHED();
        if constexpr (fetch) {
            if constexpr (cn < 8 && un == 0) xf[2 * cn + ssn][0] = lds_frag(bx[ssn]);
            if constexpr (cn < 8 && un == 3) {
                const bf16x8 gv = lds_frag(gwa + cn * 128);
                wlo[nn & 3] = __builtin_bit_cast(s16x4, __builtin_shufflevector(gv, gv, 0, 1, 2, 3));
                whi[nn & 3] = __builtin_bit_cast(s16x4, __builtin_shufflevector(gv, gv, 4, 5, 6, 7));
            } else wlo[nn & 3] = TRREAD(fad);
        }
        SCHED();
        mul(1);
        SCHED();
        if constexpr (fetch) {
            if constexpr (cn < 8 && un == 0) xf[2 * cn + ssn][1] = lds_frag(bx[ssn] + 2048);
            if constexpr (!(cn < 8 && un == 3)) whi[nn & 3] = TRREAD(fad + 784);
            constexpr int pc = P1Plan::piece_at(nn);
            if constexpr (pc >= 0) issue(pc / 4, pc % 4);
        }
        SCHED();
        if constexpr (n == P1Plan::first(P1Plan::pos(7)) + 6 + 2) {
            const float sc = cg < 8 ? QSCALE : 1.f;
            const int slot = cg < 8 ? lane : ((lane & 35) | ((lane & 16) >> 2) | ((lane & 12) << 1));
            const unsigned base = (cg < 8 ? WS_Q : WS_K) + ((4 * (cg & 7) + 2 * nh) * 64 + tile) * 1024;
#pragma unroll
            for (int e = 0; e < 2; ++e) st_ws(ws, slot * 16, base + e * 65536, join16(aq[e][0] * sc, aq[e][1] * sc));
            const int vslot = (lane & 51) | ((lane & 4) << 1) | ((lane & 8) >> 1);
            st_ws(ws, vslot * 16, WS_V + ((2 * cg + nh) * 64 + tile) * 1024,
                  (u32x4){pk2(av[0][0], av[0][1]), pk2(av[0][2], av[0][3]), pk2(av[1][0], av[1][1]), pk2(av[1][2], av[1][3])});
            *(LAS f32x2*)(size_t)(lds0 + LDS_XCH + w * 512 + lane * 8) = (f32x2){ag[0][0], ag[1][0]};
            SCHED();
        }
        if constexpr (fetch && jn == P1Plan::pos(7) + 3 && kn == 1) {
            const f32x2 o = *(const LAS f32x2*)(size_t)(lds0 + LDS_XCH + (w ^ 4) * 512 + lane * 8);
            z0 = 1.4426950408889634f * (ag[0][0] + o[0]);
            z1 = 1.4426950408889634f * (ag[1][0] + o[1]);
            SCHED();
        }
    });
    STAMP(18);
    constexpr float C0 = -2.f * 0.7978845608028654f * 1.4426950408889634f, C1 = C0 * 0.044715f;
    const float io[2] = {1.f + __builtin_amdgcn_exp2f(fminf(z0, 126.f)), 1.f + __builtin_amdgcn_exp2f(fminf(z1, 126.f))};
#pragma unroll
    for (int u = 0; u < 3; ++u) {
        f32x4 hv[2];
#pragma unroll
        for (int m = 0; m < 2; ++m)
#pragma unroll
            for (int i = 0; i < 4; i += 2) {
                const f32x2 t = {af[2 * u][m][i], af[2 * u][m][i + 1]}, up = {af[2 * u + 1][m][i], af[2 * u + 1][m][i + 1]};
                const f32x2 a = t * __builtin_elementwise_fma(t * t, (f32x2){C1, C1}, (f32x2){C0, C0});
                const f32x2 e = {__builtin_amdgcn_exp2f(a[0]), __builtin_amdgcn_exp2f(a[1])};
                const f32x2 d = __builtin_elementwise_fma(e, (f32x2){io[m], io[m]}, (f32x2){io[m], io[m]});
                const f32x2 hh = (t * up) * (f32x2){__builtin_amdgcn_rcpf(d[0]), __builtin_amdgcn_rcpf(d[1])};
                hv[m][i] = hh[0]; hv[m][i + 1] = hh[1];
            }
        const u32x4 p = join16(hv[0], hv[1]);
        const int unit = ustart + 3 * nh + u;
        if (3 * nh + u < nunit)
            st_ws(ws, (g4 & 1) * NKS * 1024 + (g4 >> 1) * 256 + r16 * 16, WS_A + (2 * tile * NKS + (unit >> 1)) * 1024 + (unit & 1) * 512, p);
    }
    return __builtin_amdgcn_rcpf(1.f + __builtin_amdgcn_exp2f(-((g4 & 1) ? z1 : z0)));
}

DI void attn_keys_safe(rsrc_t ws, const bf16x8 (&qf)[4], const float (&mx)[4], f32x16 (&o)[4], unsigned kb, unsigned vb, int lane) {
    const f32x16 zero = {};
    const unsigned cst = (lane & 11) == 0 ? 0x3F803F80u : 0u, vv = (lane >> 5) * 256 + (lane & 15) * 16;
    const bf16x8 cf = __builtin_bit_cast(bf16x8, (u32x4){cst, cst, cst, cst});
#pragma unroll 1
    for (int t = 0; t < 16; ++t) {
        const bf16x8 k0 = ld_ws(ws, lane * 16, kb + t * 1024);
        bf16x8 va = ld_ws(ws, vv, vb + t * 1024), vc = ld_ws(ws, vv, vb + t * 1024 + 512);
        if (lane & 16) { va = cf; vc = cf; }
#pragma unroll
        for (int i = 0; i < 4; ++i) {
            f32x16 s = MFMA(k0, qf[i], zero);
#pragma unroll
            for (int j = 0; j < 16; ++j) s[j] = __builtin_amdgcn_exp2f(s[j] - mx[i]);
            o[i] = SMFMAC(va, __builtin_shufflevector(s, s, 0, 1, 2, 3, 4, 5, 6, 7), o[i]);
            o[i] = SMFMAC(vc, __builtin_shufflevector(s, s, 8, 9, 10, 11, 12, 13, 14, 15), o[i]);
        }
    }
}
#define A_EXP(S, i) "v_exp_f32 v[" #S "+" #i "], v[" #S "+" #i "]\n\t"
#define A_TILE(S, T, OP, VBP, KN, QN, OC, VAC, EXTRA, WAIT)                                              \
    A_EXP(S, 0) A_EXP(S, 1)                                                                              \
    "v_smfmac_f32_32x32x32_bf16 v[" #OP ":" #OP "+15], v[" #VBP ":" #VBP "+3], v[" #T "+8:" #T "+15], v160\n\t" \
    EXTRA A_EXP(S, 2) A_EXP(S, 3) A_EXP(S, 4) WAIT                                                       \
    "v_mfma_f32_32x32x16_bf16 v[" #T ":" #T "+15], v[" #KN ":" #KN "+3], v[" #QN ":" #QN "+3], 0\n\t"    \
    A_EXP(S, 5) A_EXP(S, 6) A_EXP(S, 7) A_EXP(S, 8) A_EXP(S, 9)                                          \
    "v_smfmac_f32_32x32x32_bf16 v[" #OC ":" #OC "+15], v[" #VAC ":" #VAC "+3], v[" #S ":" #S "+7], v160\n\t" \
    A_EXP(S, 10) A_EXP(S, 11) A_EXP(S, 12) A_EXP(S, 13) A_EXP(S, 14) A_EXP(S, 15)
#define A_KEYT(KC, VA, VB, VBP, KN, EXTRA, WAIT)            \
    A_TILE(64, 80, 48, VBP, KC, 100, 0, VA, EXTRA, "")      \
    A_TILE(80, 64, 0, VB, KC, 104, 16, VA, "", "")          \
    A_TILE(64, 80, 16, VB, KC, 108, 32, VA, "", "")         \
    A_TILE(80, 64, 32, VB, KN, 96, 48, VA, "", WAIT)
#define A_LOAD2(R0, R1)                                                                            \
    "buffer_load_dwordx4 v[" #R0 ":" #R0 "+3], %[vo], %[srd], %[kb] offen sc1\n\t"                 \
    "buffer_load_dwordx4 v[" #R1 ":" #R1 "+3], %[vo], %[srd], %[kb] offen offset:1024 sc1\n\t"     \
    "s_mov_b64 exec, %[msk]\n\t"                                                                   \
    "buffer_load_dwordx4 v[" #R0 "+4:" #R0 "+7], %[vv], %[srd], %[kb] offen sc1\n\t"               \
    "buffer_load_dwordx4 v[" #R0 "+8:" #R0 "+11], %[vv], %[srd], %[kb] offen offset:512 sc1\n\t"   \
    "buffer_load_dwordx4 v[" #R1 "+4:" #R1 "+7], %[vv], %[srd], %[kb] offen offset:1024 sc1\n\t"   \
    "buffer_load_dwordx4 v[" #R1 "+8:" #R1 "+11], %[vv], %[srd], %[kb] offen offset:1536 sc1\n\t"  \
    "s_mov_b64 exec, -1\n\t"                                                                       \
    "s_add_u32 %[kb], %[kb], 2048\n\t"
#define A_CST8(R) "v_mov_b32 v[" #R "+4], %[cst]\n\tv_mov_b32 v[" #R "+5], %[cst]\n\tv_mov_b32 v[" #R "+6], %[cst]\n\tv_mov_b32 v[" #R "+7], %[cst]\n\t" \
                  "v_mov_b32 v[" #R "+8], %[cst]\n\tv_mov_b32 v[" #R "+9], %[cst]\n\tv_mov_b32 v[" #R "+10], %[cst]\n\tv_mov_b32 v[" #R "+11], %[cst]\n\t"
#define A_WAIT "s_waitcnt vmcnt(0)\n\t"
DI void attn_keys_fast(u32x4 srd, const bf16x8 (&qf)[4], f32x16 (&o)[4], unsigned kb, int lane) {
    unsigned it = 4;
    const unsigned cst = (lane & 27) == 16 ? 0x3F803F80u : 0u, vv = (WS_V - WS_K) + (lane >> 5) * 256 + (lane & 15) * 16;
    asm volatile(
        A_CST8(112) A_CST8(124)
        A_LOAD2(112, 124)
        A_CST8(136) A_CST8(148)
        "v_mov_b32 v160, 0xdddddddd\n\t"
        "v_mov_b32 v88, 0\n\tv_mov_b32 v89, 0\n\tv_mov_b32 v90, 0\n\tv_mov_b32 v91, 0\n\t"
        "v_mov_b32 v92, 0\n\tv_mov_b32 v93, 0\n\tv_mov_b32 v94, 0\n\tv_mov_b32 v95, 0\n\t"
        A_WAIT
        "v_mfma_f32_32x32x16_bf16 v[64:79], v[112:115], v[96:99], 0\n\t"
        "s_nop 11\n\t"
        ".p2align 3\n"
        "1:\n\t"
        A_KEYT(112, 116, 120, 156, 124, A_LOAD2(136, 148), "")
        A_KEYT(124, 128, 132, 120, 136, "", A_WAIT)
        A_KEYT(136, 140, 144, 132, 148, A_LOAD2(112, 124), "")
        A_KEYT(148, 152, 156, 144, 112, "", A_WAIT)
        "s_sub_u32 %[it], %[it], 1\n\t"
        "s_cmp_lg_u32 %[it], 0\n\t"
        "s_cbranch_scc1 1b\n\t"
        "v_smfmac_f32_32x32x32_bf16 v[48:63], v[156:159], v[88:95], v160\n\t"
        "s_nop 15"
        : "+{v[0:15]}"(o[0]), "+{v[16:31]}"(o[1]), "+{v[32:47]}"(o[2]), "+{v[48:63]}"(o[3]), [kb] "+s"(kb), [it] "+s"(it)
        : "{v[96:99]}"(qf[0]), "{v[100:103]}"(qf[1]), "{v[104:107]}"(qf[2]), "{v[108:111]}"(qf[3]), [vo] "v"(lane * 16), [vv] "v"(vv),
          [cst] "v"(cst), [srd] "s"(srd), [msk] "s"(0x0000FFFF0000FFFFull)
        : "memory", "scc", "v64", "v65", "v66", "v67", "v68", "v69", "v70", "v71", "v72", "v73", "v74", "v75", "v76", "v77", "v78", "v79",
          "v80", "v81", "v82", "v83", "v84", "v85", "v86", "v87", "v88", "v89", "v90", "v91", "v92", "v93", "v94", "v95",
          "v112", "v113", "v114", "v115", "v116", "v117", "v118", "v119", "v120", "v121", "v122", "v123", "v124", "v125", "v126", "v127",
          "v128", "v129", "v130", "v131", "v132", "v133", "v134", "v135", "v136", "v137", "v138", "v139", "v140", "v141", "v142", "v143",
          "v144", "v145", "v146", "v147", "v148", "v149", "v150", "v151", "v152", "v153", "v154", "v155", "v156", "v157", "v158", "v159", "v160");
}
DI float attn_reduce(unsigned red, const f32x16 (&o)[4], f32x8& oo, int e, int kq, int lane) {
    float l = o[0][8];
#pragma unroll
    for (int j = 0; j < 8; ++j) oo[j] = o[0][j];
#pragma unroll
    for (int i = 1; i < 4; ++i) {
        const unsigned a = red + ((e * 4 + ((i + kq) & 3)) * 3 + (i - 1)) * 2304;
        *(LAS f32x4*)(size_t)(a + lane * 16) = __builtin_shufflevector(o[i], o[i], 0, 1, 2, 3);
        *(LAS f32x4*)(size_t)(a + 1024 + lane * 16) = __builtin_shufflevector(o[i], o[i], 4, 5, 6, 7);
        *(LAS float*)(size_t)(a + 2048 + lane * 4) = o[i][8];
    }
    LGKM_BAR();
#pragma unroll
    for (int i = 0; i < 3; ++i) {
        const unsigned a = red + ((e * 4 + kq) * 3 + i) * 2304;
        const f32x4 lo = *(const LAS f32x4*)(size_t)(a + lane * 16), hi = *(const LAS f32x4*)(size_t)(a + 1024 + lane * 16);
#pragma unroll
        for (int j = 0; j < 4; ++j) { oo[j] += lo[j]; oo[4 + j] += hi[j]; }
        l += *(const LAS float*)(size_t)(a + 2048 + lane * 4);
    }
    return l;
}
DI void phase2(const Ctx& c, unsigned lds0, int rb, int hp, int w, int lane, float g STM_ARG) {
    const rsrc_t ws = make_rsrc(c.ws, WS_END);
    const int e = w >> 2, kq = w & 3, head = 2 * hp + e;
    const unsigned red = lds0, flg = lds0 + 24 * 2304;
    bf16x8 qf[4];
#pragma unroll
    for (int i = 0; i < 4; ++i) qf[i] = ld_ws(ws, lane * 16, WS_Q + (head * 64 + rb * 4 + ((i + kq) & 3)) * 1024);
    const unsigned kb = WS_K + (head * 64 + kq * 16) * 1024;
    f32x16 o[4] = {};
    STAMP(1);
    attn_keys_fast(make_srd(c.ws, WS_END), qf, o, kb, lane);
    STAMP(2);
    f32x8 oo;
    float l = attn_reduce(red, o, oo, e, kq, lane);
    float am = 0.f;
#pragma unroll
    for (int j = 0; j < 8; ++j) am = fmaxf(am, fabsf(oo[j]));
    const bool ok = l >= 0x1p-40f && l <= 0x1p40f && am <= 0x1p120f;
    const int bad = __any(!ok);
    if (lane == 0) *(LAS int*)(size_t)(flg + w * 4) = bad;
    LGKM_BAR();
    const u32x4 f0 = *(const LAS u32x4*)(size_t)flg, f1 = *(const LAS u32x4*)(size_t)(flg + 16);
    if (__builtin_amdgcn_readfirstlane(f0[0] | f0[1] | f0[2] | f0[3] | f1[0] | f1[1] | f1[2] | f1[3])) {
        const f32x16 zero = {};
        float mx[4] = {-INFINITY, -INFINITY, -INFINITY, -INFINITY};
#pragma unroll 1
        for (int t = 0; t < 64; ++t) {
            const bf16x8 k0 = ld_ws(ws, lane * 16, WS_K + (head * 64 + t) * 1024);
#pragma unroll
            for (int i = 0; i < 4; ++i) {
                const f32x16 s = MFMA(k0, qf[i], zero);
#pragma unroll
                for (int j = 0; j < 16; ++j) mx[i] = fmaxf(mx[i], s[j]);
            }
        }
#pragma unroll
        for (int i = 0; i < 4; ++i) { mx[i] = half_max(mx[i]); o[i] = zero; }
        attn_keys_safe(ws, qf, mx, o, kb, kb + (WS_V - WS_K), lane);
        l = attn_reduce(red, o, oo, e, kq, lane);
    }
    const float sc = g * __builtin_amdgcn_rcpf(l);
    const u32x4 p = {pk2(oo[0] * sc, oo[1] * sc), pk2(oo[2] * sc, oo[3] * sc), pk2(oo[4] * sc, oo[5] * sc), pk2(oo[6] * sc, oo[7] * sc)};
    st_ws(ws, ((lane >> 4) & 1) * NKS * 1024 + (lane >> 5) * 256 + (lane & 15) * 16, WS_A + (2 * (rb * 4 + kq) * NKS + 43 + hp) * 1024 + e * 512, p);
}

DI void phase3(const Ctx& c, unsigned lds0, int rt, int cb, int w, int lane STM_ARG) {
    const rsrc_t ws = make_rsrc(c.ws, WS_END);
    const u32x4 sd = make_srd(c.wd, FF * DM * 2), so = make_srd(c.wo, DM * DM * 2);
    const int mt = w & 1, kq = w >> 1, r16 = lane & 15, g4 = lane >> 4;
    const unsigned vw0 = (16 * w + (lane >> 3)) * 1024 + cb * 128 + (((lane & 7) ^ ((g4 & 1) << 1)) << 4), vw1 = (vw0 ^ 64) + 8192;
    auto issue = [&](int st) {
        const unsigned sl = lds0 + (st % 9) * 16384 + w * 2048;
        const unsigned row0 = (st < 11 ? st : st - 11) * 128 * 1024;
        dma16(st < 11 ? sd : so, vw0, row0, sl);
        dma16(st < 11 ? sd : so, vw1, row0, sl + 1024);
    };
    const unsigned ab = WS_A + ((rt * 4 + mt * 2) * NKS + kq) * 1024;
    bf16x8 a[15][2];
#pragma unroll
    for (int st = 0; st < 15; ++st) {
        if (st < 9) issue(st);
        const int ks = st < 11 ? st * 4 : st * 4 - 1;
#pragma unroll
        for (int m = 0; m < 2; ++m) a[st][m] = ld_ws(ws, lane * 16, ab + (m * NKS + ks) * 1024);
        SCHED();
    }
    if (kq == 3) { a[10][0] = bf16x8{}; a[10][1] = bf16x8{}; }
    const unsigned wb = lds0 + (32 * kq + 8 * g4 + (r16 >> 2)) * 128 + (((r16 >> 3) | ((g4 & 1) << 1)) << 5) + 8 * (lane & 3);
    f32x4 acc[4][2] = {};
    auto compute = [&](int st) {
#pragma unroll
        for (int nu = 0; nu < 4; ++nu) {
            const bf16x8 wv = lds_frag_tr((wb ^ (nu * 32)) + (st % 9) * 16384, 512);
            acc[nu][0] = MFMA16(wv, a[st][0], acc[nu][0]);
            acc[nu][1] = MFMA16(wv, a[st][1], acc[nu][1]);
        }
    };
    STAMP(1);
    WAIT_BAR(0);
    STAMP(2);
    compute(0); compute(1); compute(2);
    LGKM_BAR();
    STAMP(3);
    issue(9); issue(10); issue(11);
    compute(3); compute(4); compute(5);
    LGKM_BAR();
    issue(12); issue(13); issue(14);
    compute(6); compute(7); compute(8);
    WAIT_BAR(0);
    STAMP(4);
    compute(9); compute(10); compute(11); compute(12); compute(13); compute(14);
    LGKM_BAR();
    STAMP(5);
#pragma unroll
    for (int nu = 0; nu < 4; ++nu)
#pragma unroll
        for (int m = 0; m < 2; ++m)
            *(LAS f32x4*)(size_t)(lds0 + (kq * 64 + mt * 32 + m * 16 + r16) * 272 + (nu * 16 + 4 * g4) * 4) = acc[nu][m];
    LGKM_BAR();
    const int row = w * 8 + (lane >> 3), c8 = lane & 7;
    f32x4 lo = {}, hi = {};
#pragma unroll
    for (int q = 0; q < 4; ++q) {
        const unsigned ad = lds0 + (q * 64 + row) * 272 + c8 * 32;
        lo += *(const LAS f32x4*)(size_t)ad;
        hi += *(const LAS f32x4*)(size_t)(ad + 16);
    }
    const u32x4 p = {pk2(lo[0], lo[1]), pk2(lo[2], lo[3]), pk2(hi[0], hi[1]), pk2(hi[2], hi[3])};
    *(u32x4*)((char*)c.out + (rt * 64 + row) * 1024 + cb * 128 + c8 * 16) = p;
}

template <int PH>
__global__ __launch_bounds__(512, 2) void gm_kernel(const void* x, const void* wqkv, const void* wo, const void* wg, const void* wu,
                                                    const void* wd, const void* gw, void* out, void* ws) {
    extern __shared__ __attribute__((aligned(16))) char smem[];
    const unsigned lds0 = (unsigned)(size_t)(LAS char*)smem;
    const Ctx c = {x, wqkv, wo, wg, wu, wd, gw, out, ws};
    const int lane = threadIdx.x & 63, w = __builtin_amdgcn_readfirstlane(threadIdx.x >> 6);
    const int b = blockIdx.x, xcd = b & 7, j = b >> 3;
    const int rb = (xcd >> 2) * 8 + (j & 7), cg = (xcd & 3) * 4 + (j >> 3);
    float* gp = (float*)((char*)ws + WS_G) + rb * 128 + (w & 3) * 32 + (lane & 31);
    STAMP(0);
    if (PH == 1) { const float g = phase1(c, lds0, rb, cg, w, lane STM_PASS); if (cg == 0 && w < 4 && lane < 32) *gp = g; }
    if (PH == 2) phase2(c, lds0, rb, cg, w, lane, *gp STM_PASS);
    if (PH == 3) phase3(c, lds0, xcd * 4 + (j >> 3), j & 7, w, lane STM_PASS);
}

extern "C" void kernel_launch(void* const* d_in, const int* in_sizes, int n_in, void* d_out, int out_size, void* d_ws, size_t ws_size,
                              hipStream_t stream) {
    (void)hipFuncSetAttribute((const void*)gm_kernel<1>, hipFuncAttributeMaxDynamicSharedMemorySize, LDS_BYTES);
    (void)hipFuncSetAttribute((const void*)gm_kernel<2>, hipFuncAttributeMaxDynamicSharedMemorySize, LDS_BYTES);
    (void)hipFuncSetAttribute((const void*)gm_kernel<3>, hipFuncAttributeMaxDynamicSharedMemorySize, LDS_BYTES);
    gm_kernel<1><<<256, 512, LDS_BYTES, stream>>>(d_in[0], d_in[1], d_in[2], d_in[4], d_in[5], d_in[6], d_in[7], d_out, d_ws);
    gm_kernel<2><<<256, 512, LDS_BYTES, stream>>>(d_in[0], d_in[1], d_in[2], d_in[4], d_in[5], d_in[6], d_in[7], d_out, d_ws);
    gm_kernel<3><<<256, 512, LDS_BYTES, stream>>>(d_in[0], d_in[1], d_in[2], d_in[4], d_in[5], d_in[6], d_in[7], d_out, d_ws);
}
```

```cpp
#include <hip/hip_runtime.h>
#include <hip/hip_bf16.h>

typedef float f32x2 __attribute__((ext_vector_type(2)));
typedef float f32x4 __attribute__((ext_vector_type(4)));
typedef float f32x8 __attribute__((ext_vector_type(8)));
typedef float f32x16 __attribute__((ext_vector_type(16)));
typedef __bf16 bf16x2 __attribute__((ext_vector_type(2)));
typedef __bf16 bf16x8 __attribute__((ext_vector_type(8)));
typedef __bf16 bf16x16 __attribute__((ext_vector_type(16)));
typedef short s16x4 __attribute__((ext_vector_type(4)));
typedef unsigned u32x2 __attribute__((ext_vector_type(2)));
typedef unsigned u32x4 __attribute__((ext_vector_type(4)));
#define LAS __attribute__((address_space(3)))
#define DI __device__ __forceinline__

constexpr int SEQ = 2048, DM = 512, FF = 1376;
constexpr int NKS = 59;
constexpr unsigned WS_K = 0, WS_V = 2u << 20, WS_A = 4u << 20, WS_F1 = 12u << 20, WS_FH = WS_F1 + 16384, WS_FO = WS_FH + 16384,
                   WS_EP = WS_FO + 16384, WS_END = WS_EP + 256;
constexpr int WPART = 16 * 784;
constexpr int SLOT = 16384 + WPART, LDS_XCH = 5 * SLOT, LDS_GW = LDS_XCH + 8 * 512, LDS_DUMMY = LDS_GW + 1024, LDS_Q = LDS_DUMMY + 256;
constexpr int LDS_RED = 6 * 16384, LDS_FLG = LDS_RED + 24 * 2304, LDS_BYTES = 160 * 1024;
static_assert(LDS_Q + 8192 <= LDS_BYTES && LDS_FLG + 32 <= LDS_BYTES, "LDS map");
constexpr float LOG2E = 1.4426950408889634f, QSCALE = 0.25f * LOG2E;
constexpr int SPIN_CAP = 1 << 16;
typedef unsigned long long u64;

DI u32x4 make_srd(const void* p, unsigned bytes) {
    const unsigned long long a = (unsigned long long)p;
    return (u32x4){(unsigned)a, (unsigned)(a >> 32) & 0xffffu, bytes, 0x00020000u};
}
DI void dma16(u32x4 srd, unsigned voff, unsigned soff, unsigned lds) {
    asm volatile("s_mov_b32 m0, %0\n\ts_nop 0\n\tbuffer_load_dwordx4 %1, %2, %3 offen lds" ::"s"(lds), "v"(voff), "s"(srd), "s"(soff) : "memory");
}
DI void dma16_masked(u32x4 srd, unsigned voff, unsigned soff, unsigned lds, unsigned long long mask) {
    asm volatile("s_mov_b32 m0, %0\n\ts_mov_b64 exec, %4\n\tbuffer_load_dwordx4 %1, %2, %3 offen lds\n\ts_mov_b64 exec, -1"
                 ::"s"(lds), "v"(voff), "s"(srd), "s"(soff), "s"(mask) : "memory");
}
template <int N> DI void vm_bar() { asm volatile("s_waitcnt vmcnt(%0)\n\ts_barrier" ::"i"(N) : "memory"); }
#define SCHED() __builtin_amdgcn_sched_barrier(0)
template <int I> struct IC { static constexpr int v = I; };
template <int B, int E, class Fn> DI void static_for(Fn&& f) { if constexpr (B < E) { f(IC<B>{}); static_for<B + 1, E>(f); } }
#define WAIT_BAR(N) asm volatile("s_waitcnt vmcnt(" #N ") lgkmcnt(0)\n\ts_barrier" ::: "memory")
#define LGKM_BAR() asm volatile("s_waitcnt lgkmcnt(0)\n\ts_barrier" ::: "memory")
template <int N> DI void wait_bar() { asm volatile("s_waitcnt vmcnt(%0) lgkmcnt(0)\n\ts_barrier" ::"i"(N) : "memory"); }

#if __HIP_DEVICE_COMPILE__
#define MFMA(a, b, c) __builtin_amdgcn_mfma_f32_32x32x16_bf16((a), (b), (c), 0, 0, 0)
#define SMFMAC(a, b, c) __builtin_amdgcn_smfmac_f32_32x32x32_bf16((a), __builtin_bit_cast(bf16x16, (b)), (c), (int)0xDDDDDDDD, 0, 0)
#define MFMA16(a, b, c) __builtin_amdgcn_mfma_f32_16x16x32_bf16((a), (b), (c), 0, 0, 0)
#define TRREAD(a) __builtin_amdgcn_ds_read_tr16_b64_v4i16((LAS s16x4*)(size_t)(a))
#else
#define MFMA(a, b, c) (c)
#define SMFMAC(a, b, c) (c)
#define MFMA16(a, b, c) (c)
#define TRREAD(a) (s16x4{})
#endif
DI bf16x8 lds_frag(unsigned a) { return *(const LAS bf16x8*)(size_t)a; }
DI bf16x8 lds_frag_tr(unsigned a, int step) {
#if __HIP_DEVICE_COMPILE__
    const s16x4 lo = __builtin_amdgcn_ds_read_tr16_b64_v4i16((LAS s16x4*)(size_t)a);
    const s16x4 hi = __builtin_amdgcn_ds_read_tr16_b64_v4i16((LAS s16x4*)(size_t)(a + step));
    return __builtin_bit_cast(bf16x8, __builtin_shufflevector(lo, hi, 0, 1, 2, 3, 4, 5, 6, 7));
#else
    return bf16x8{};
#endif
}
DI unsigned pk2(float a, float b) { return __builtin_bit_cast(unsigned, __builtin_convertvector((f32x2){a, b}, bf16x2)); }
DI bf16x8 cat8(s16x4 lo, s16x4 hi) { return __builtin_bit_cast(bf16x8, __builtin_shufflevector(lo, hi, 0, 1, 2, 3, 4, 5, 6, 7)); }
DI u32x4 join16(f32x4 a, f32x4 b) {
    auto r0 = __builtin_amdgcn_permlane16_swap(pk2(a[0], a[1]), pk2(b[0], b[1]), false, false);
    auto r1 = __builtin_amdgcn_permlane16_swap(pk2(a[2], a[3]), pk2(b[2], b[3]), false, false);
    return (u32x4){r0[0], r1[0], r0[1], r1[1]};
}
DI float half_max(float v) {
    auto r = __builtin_amdgcn_permlane32_swap(__float_as_uint(v), __float_as_uint(v), false, false);
    return fmaxf(__uint_as_float(r[0]), __uint_as_float(r[1]));
}
typedef __amdgpu_buffer_rsrc_t rsrc_t;
DI rsrc_t make_rsrc(const void* p, unsigned bytes) { return __builtin_amdgcn_make_buffer_rsrc((void*)p, 0, bytes, 0x00020000); }
constexpr int AUX_DEV = 16;
DI void st_ws(rsrc_t ws, unsigned voff, unsigned soff, u32x4 v) { __builtin_amdgcn_raw_buffer_store_b128(v, ws, voff + soff, 0, AUX_DEV); }
DI bf16x8 ld_ws(rsrc_t ws, unsigned voff, unsigned soff) { return __builtin_bit_cast(bf16x8, __builtin_amdgcn_raw_buffer_load_b128(ws, voff, soff, AUX_DEV)); }

DI void post_tag(rsrc_t ws, unsigned off, u64 tag, int lane) {
    if (lane == 0) __builtin_amdgcn_raw_buffer_store_b64((u32x2){(unsigned)tag, (unsigned)(tag >> 32)}, ws, off, 0, AUX_DEV);
}
DI void wait_tags(rsrc_t ws, unsigned voff, u64 tag) {
#pragma unroll 1
    for (int spin = 0; spin < SPIN_CAP; ++spin) {
        asm volatile("" ::: "memory");
        const u32x2 v = __builtin_amdgcn_raw_buffer_load_b64(ws, voff, 0, AUX_DEV);
        if (__all(v[0] == (unsigned)tag && v[1] == (unsigned)(tag >> 32))) break;
        __builtin_amdgcn_s_sleep(1);
    }
    asm volatile("" ::: "memory");
}

#define STAMP(i)

#define P1_ORDER 0, 1, 2, 3, 4, 5, 6, 7, 8, 9, 10, 11, 12, 13, 14, 15
#define P1_PRO 12
#define P1_JF 11
namespace P1Plan {
    constexpr int order[16] = {P1_ORDER};
    constexpr int PD = 3;
    constexpr int cnt(int j) { return order[j] < 8 ? 7 : 12; }
    constexpr int first(int j) { int n = 0; for (int i = 0; i < j; ++i) n += cnt(i); return n; }
    constexpr int stage(int n) { int j = 0; while (j < 15 && first(j + 1) <= n) ++j; return j; }
    constexpr int pos(int c) { int j = 0; while (order[j] != c) ++j; return j; }
    constexpr int total = 8 * 7 + 8 * 12;
    constexpr int PRO = P1_PRO, PACE = 2;
    constexpr int when(int p) {
        int t = -PACE;
        for (int q = PRO; q <= p; ++q) { const int lo = q / 4 >= 5 ? first(q / 4 - 3) : 0; t = t + PACE > lo ? t + PACE : lo; }
        return t;
    }
    constexpr int piece_at(int n) { for (int p = PRO; p < 64; ++p) if (when(p) == n) return p; return -1; }
    constexpr int need(int j) { return j == 14 ? 15 : j; }
    constexpr int ST = first(pos(7)) + 7 + 1 + PD;
    constexpr int JF = P1_JF;
    constexpr int vm(int j) {
        const int last = 4 * need(j) + 3;
        int n = 0, y = 0;
        for (int p = last + 1; p < 64; ++p) if (when(p) < first(j)) ++n;
        for (int p = PRO; p < 64; ++p) if (when(p) > ST && when(p) < first(j)) ++y;
        if (j == JF) return n < y ? n : y;
        if (j < JF && ST < first(j) && ST >= when(last)) n += 2;
        if (j > JF && first(JF) > when(last)) n += 1;
        return n;
    }
}
static_assert(P1Plan::ST < P1Plan::first(P1Plan::JF) && P1Plan::JF < 15, "P1 plan");

DI void attn_keys_safe(rsrc_t ws, const bf16x8 (&qf)[4], const float (&mx)[4], f32x16 (&o)[4], unsigned kb, unsigned vb, int lane) {
    const f32x16 zero = {};
    const unsigned cst = (lane & 11) == 0 ? 0x3F803F80u : 0u, vv = (lane >> 5) * 256 + (lane & 15) * 16;
    const bf16x8 cf = __builtin_bit_cast(bf16x8, (u32x4){cst, cst, cst, cst});
#pragma unroll 1
    for (int t = 0; t < 16; ++t) {
        const bf16x8 k0 = ld_ws(ws, lane * 16, kb + t * 1024);
        bf16x8 va = ld_ws(ws, vv, vb + t * 1024), vc = ld_ws(ws, vv, vb + t * 1024 + 512);
        if (lane & 16) { va = cf; vc = cf; }
#pragma unroll
        for (int i = 0; i < 4; ++i) {
            f32x16 s = MFMA(k0, qf[i], zero);
#pragma unroll
            for (int j = 0; j < 16; ++j) s[j] = __builtin_amdgcn_exp2f(s[j] - mx[i]);
            o[i] = SMFMAC(va, __builtin_shufflevector(s, s, 0, 1, 2, 3, 4, 5, 6, 7), o[i]);
            o[i] = SMFMAC(vc, __builtin_shufflevector(s, s, 8, 9, 10, 11, 12, 13, 14, 15), o[i]);
        }
    }
}
#define A_EXP(S, i) "v_exp_f32 v[" #S "+" #i "], v[" #S "+" #i "]\n\t"
#define A_TILE(S, T, OP, VBP, KN, QN, OC, VAC, EXTRA, WAIT)                                              \
    A_EXP(S, 0) A_EXP(S, 1)                                                                              \
    "v_smfmac_f32_32x32x32_bf16 v[" #OP ":" #OP "+15], v[" #VBP ":" #VBP "+3], v[" #T "+8:" #T "+15], v160\n\t" \
    EXTRA A_EXP(S, 2) A_EXP(S, 3) A_EXP(S, 4) WAIT                                                       \
    "v_mfma_f32_32x32x16_bf16 v[" #T ":" #T "+15], v[" #KN ":" #KN "+3], v[" #QN ":" #QN "+3], 0\n\t"    \
    A_EXP(S, 5) A_EXP(S, 6) A_EXP(S, 7) A_EXP(S, 8) A_EXP(S, 9)                                          \
    "v_smfmac_f32_32x32x32_bf16 v[" #OC ":" #OC "+15], v[" #VAC ":" #VAC "+3], v[" #S ":" #S "+7], v160\n\t" \
    A_EXP(S, 10) A_EXP(S, 11) A_EXP(S, 12) A_EXP(S, 13) A_EXP(S, 14) A_EXP(S, 15)
#define A_KEYT(KC, VA, VB, VBP, KN, E0, E1, E2, WAIT)       \
    A_TILE(64, 80, 48, VBP, KC, 100, 0, VA, E0, "")         \
    A_TILE(80, 64, 0, VB, KC, 104, 16, VA, E1, "")          \
    A_TILE(64, 80, 16, VB, KC, 108, 32, VA, E2, "")         \
    A_TILE(80, 64, 32, VB, KN, 96, 48, VA, "", WAIT)
#define A_T0(E0, E1, E2) A_KEYT(112, 116, 120, 156, 124, E0, E1, E2, "")
#define A_T1(E0, E1, E2, W) A_KEYT(124, 128, 132, 120, 136, E0, E1, E2, W)
#define A_T2(E0, E1, E2) A_KEYT(136, 140, 144, 132, 148, E0, E1, E2, "")
#define A_T3(E0, E1, E2, W) A_KEYT(148, 152, 156, 144, 112, E0, E1, E2, W)
#define A_LOAD2(R0, R1)                                                                            \
    "buffer_load_dwordx4 v[" #R0 ":" #R0 "+3], %[vo], %[srd], %[kb] offen sc1\n\t"                 \
    "buffer_load_dwordx4 v[" #R1 ":" #R1 "+3], %[vo], %[srd], %[kb] offen offset:1024 sc1\n\t"     \
    "s_mov_b64 exec, %[msk]\n\t"                                                                   \
    "buffer_load_dwordx4 v[" #R0 "+4:" #R0 "+7], %[vv], %[srd], %[kb] offen sc1\n\t"               \
    "buffer_load_dwordx4 v[" #R0 "+8:" #R0 "+11], %[vv], %[srd], %[kb] offen offset:512 sc1\n\t"   \
    "buffer_load_dwordx4 v[" #R1 "+4:" #R1 "+7], %[vv], %[srd], %[kb] offen offset:1024 sc1\n\t"   \
    "buffer_load_dwordx4 v[" #R1 "+8:" #R1 "+11], %[vv], %[srd], %[kb] offen offset:1536 sc1\n\t"  \
    "s_mov_b64 exec, -1\n\t"                                                                       \
    "s_add_u32 %[kb], %[kb], 2048\n\t"
#define A_L01 A_LOAD2(112, 124)
#define A_L23 A_LOAD2(136, 148)
#define A_CST8(R) "v_mov_b32 v[" #R "+4], %[cst]\n\tv_mov_b32 v[" #R "+5], %[cst]\n\tv_mov_b32 v[" #R "+6], %[cst]\n\tv_mov_b32 v[" #R "+7], %[cst]\n\t" \
                  "v_mov_b32 v[" #R "+8], %[cst]\n\tv_mov_b32 v[" #R "+9], %[cst]\n\tv_mov_b32 v[" #R "+10], %[cst]\n\tv_mov_b32 v[" #R "+11], %[cst]\n\t"
#define A_W(N) "s_waitcnt vmcnt(" #N ")\n\t"
#define A_D0 "s_mov_b32 m0, %[wl]\n\ts_add_u32 %[wl], %[wl], 1024\n\tbuffer_load_dwordx4 %[vw0], %[sd], %[wo] offen lds\n\t"
#define A_D1 "s_mov_b32 m0, %[wl]\n\ts_add_u32 %[wl], %[wl], 15360\n\tbuffer_load_dwordx4 %[vw1], %[sd], %[wo] offen lds\n\ts_add_u32 %[wo], %[wo], 131072\n\t"
#define A_H(R) "buffer_load_dwordx4 v[" #R ":" #R "+3], %[vo], %[srd], %[hb] offen sc1\n\ts_add_u32 %[hb], %[hb], 60416\n\t" \
               "buffer_load_dwordx4 v[" #R "+4:" #R "+7], %[vo], %[srd], %[hb] offen sc1\n\ts_sub_u32 %[hb], %[hb], 56320\n\t"
static_assert(NKS * 1024 == 60416 && 60416 - 4096 == 56320, "A_H strides");
#define A_POST "v_mov_b32 v161, %[fh]\n\tv_mov_b32 v162, %[tg0]\n\tv_mov_b32 v163, %[tg1]\n\ts_mov_b64 exec, 1\n\t" \
               "buffer_store_dwordx2 v[162:163], v161, %[srd], 0 offen sc1\n\ts_mov_b64 exec, -1\n\t"
#define A_POLL "v_and_b32 v161, 0x1f0, %[vo]\n\tv_lshrrev_b32 v161, 1, v161\n\tbuffer_load_dwordx2 v[162:163], v161, %[srd], %[fp] offen sc1\n\t"
#define A_CHECK "2:\n\tv_cmp_ne_u32 vcc, %[tg0], v162\n\tv_cmp_ne_u32 %[tmp], %[tg1], v163\n\ts_or_b64 vcc, vcc, %[tmp]\n\ts_cbranch_scc0 3f\n\t" \
                "s_sub_u32 %[cap], %[cap], 1\n\ts_cbranch_scc1 3f\n\ts_sleep 1\n\t"                                                             \
                "buffer_load_dwordx2 v[162:163], v161, %[srd], %[fp] offen sc1\n\ts_waitcnt vmcnt(0)\n\ts_branch 2b\n3:\n\t"
typedef unsigned u32x16 __attribute__((ext_vector_type(16)));
typedef unsigned u32x8 __attribute__((ext_vector_type(8)));
DI float attn_reduce(unsigned red, const f32x16 (&o)[4], f32x8& oo, int e, int kq, int lane) {
    float l = o[0][8];
#pragma unroll
    for (int j = 0; j < 8; ++j) oo[j] = o[0][j];
#pragma unroll
    for (int i = 1; i < 4; ++i) {
        const unsigned a = red + ((e * 4 + ((i + kq) & 3)) * 3 + (i - 1)) * 2304;
        *(LAS f32x4*)(size_t)(a + lane * 16) = __builtin_shufflevector(o[i], o[i], 0, 1, 2, 3);
        *(LAS f32x4*)(size_t)(a + 1024 + lane * 16) = __builtin_shufflevector(o[i], o[i], 4, 5, 6, 7);
        *(LAS float*)(size_t)(a + 2048 + lane * 4) = o[i][8];
    }
    LGKM_BAR();
#pragma unroll
    for (int i = 0; i < 3; ++i) {
        const unsigned a = red + ((e * 4 + kq) * 3 + i) * 2304;
        const f32x4 lo = *(const LAS f32x4*)(size_t)(a + lane * 16), hi = *(const LAS f32x4*)(size_t)(a + 1024 + lane * 16);
#pragma unroll
        for (int j = 0; j < 4; ++j) { oo[j] += lo[j]; oo[4 + j] += hi[j]; }
        l += *(const LAS float*)(size_t)(a + 2048 + lane * 4);
    }
    return l;
}

__global__ __launch_bounds__(512, 2) void gm_kernel(const void* px, const void* pwqkv, const void* pwo, const void* pwg, const void* pwu,
                                                    const void* pwd, const void* pgw, void* pout, void* pws) {
    extern __shared__ __attribute__((aligned(16))) char smem[];
    const unsigned lds0 = (unsigned)(size_t)(LAS char*)smem;
    int lane = threadIdx.x & 63;
    const int w = __builtin_amdgcn_readfirstlane(threadIdx.x >> 6);
    const int b = blockIdx.x, xcd = b & 7, jb = b >> 3;
    const int rb = (xcd >> 2) * 8 + (jb & 7), cg = (xcd & 3) * 4 + (jb >> 3);
    const int rt = xcd * 4 + (jb >> 3), cb = jb & 7;
    STAMP(0);
    int r16 = lane & 15, g4 = lane >> 4;
    const rsrc_t ws = make_rsrc(pws, WS_END);
    const u32x4 sws = make_srd(pws, WS_END);
    const int mt = w & 3, nh = w >> 2, tile = rb * 4 + mt;
    const u32x4 sx = make_srd(px, SEQ * DM * 2), sq = make_srd(pwqkv, DM * 3 * DM * 2);
    const u32x4 sg = make_srd(pwg, DM * FF * 2), su = make_srd(pwu, DM * FF * 2), sw = make_srd(pgw, DM * 2);
    {
        const int per = mt == 0 ? 32 : mt == 1 ? 24 : 22;
        const u32x4 sr = mt == 0 ? sx : mt == 1 ? sq : mt == 2 ? sg : su;
        const unsigned a = lane < per ? ((2 * b + nh) * per + lane) * 128 : 0x7fffff00u;
        asm volatile("s_mov_b32 m0, %0\n\ts_nop 0\n\tbuffer_load_dword %1, %2, 0 offen lds" ::"s"(lds0 + LDS_DUMMY), "v"(a), "s"(sr) : "memory");
    }
    u64 tag = *(const __attribute__((address_space(4))) u64*)((const char*)pws + WS_EP) * 0x9E3779B97F4A7C15ull + 0xD1B54A32D192ED03ull;
    const int ustart = cg < 6 ? 6 * cg : 5 * cg + 6, nunit = cg < 6 ? 6 : 5;
    const u64 m12 = 0x0000FFFFFFFFFFFFull, mffn = cg < 6 ? m12 : 0x00003FF3FF3FF3FFull;
    const int wr = lane / 12, wc = lane - 12 * wr;
    const unsigned vx0 = (rb * 128 + 16 * w + (lane >> 3)) * 1024 + (((lane & 7) ^ (lane >> 4)) << 4);
    const unsigned vx1 = (rb * 128 + 16 * w + 8 + (lane >> 3)) * 1024 + (((lane & 7) ^ (4 + (lane >> 4))) << 4);
    const unsigned vwq = (8 * w + wr) * 3072 + (wc < 8 ? 1024 + cg * 128 + wc * 16 : cg * 64 + (wc - 8) * 16);
    const unsigned vwf = (8 * w + wr) * 2752 + ustart * 32 + wc * 16;
    auto issue = [&](int j, int pc) {
        const unsigned sl = lds0 + (j % 5) * SLOT;
        const int cs = P1Plan::order[j], i = pc & 1;
        if (cs < 8) {
            if (pc < 2) dma16(sx, i ? vx1 : vx0, cs * 128, sl + w * 2048 + i * 1024);
            else dma16_masked(sq, vwq, (cs * 64 + 4 * i) * 3072, sl + 16384 + (2 * w + i) * 784, m12);
        } else {
            dma16_masked(pc < 2 ? sg : su, vwf, ((cs - 8) * 64 + 4 * i) * 2752, sl + (pc < 2 ? 0 : WPART) + (2 * w + i) * 784, mffn);
        }
    };
    dma16(sw, lane * 16, 0, lds0 + LDS_GW);
#pragma unroll
    for (int j = 0; j < P1Plan::PRO / 4; ++j) {
#pragma unroll
        for (int pc = 0; pc < 4; ++pc) issue(j, pc);
        if (j + 1 < P1Plan::PRO / 4) __builtin_amdgcn_s_barrier();
    }
    asm volatile("" : "+s"(tag));
    unsigned xa[2], qa[2], va[2], fa[2];
    {
        const unsigned wb = lds0 + 2 * g4 * 784 + (r16 >> 2) * 192 + 8 * (lane & 3);
#pragma unroll
        for (int ss = 0; ss < 2; ++ss) {
            xa[ss] = lds0 + (32 * mt + r16) * 128 + (((g4 ^ (r16 >> 1) ^ (nh << 2)) ^ (4 * ss)) << 4);
            const unsigned o = wb + (ss ^ nh) * 8 * 784;
            qa[ss] = o + 16384 + 64 * nh; va[ss] = o + 16384 + 128 + 32 * nh; fa[ss] = o + 96 * nh;
        }
    }
    const unsigned gwa = lds0 + LDS_GW + 16 * g4 + 64 * nh;
    bf16x8 xf[16][2];
    s16x4 wlo[4], whi[4];
    f32x4 aq[4][2] = {}, af[6][2] = {};
    float z0 = 0.f, z1 = 0.f;
    unsigned bx[2], bq[2], bv[2];
    static_for<-P1Plan::PD, P1Plan::total>([&](auto I) {
        constexpr int n = I.v, nn = n + P1Plan::PD;
        constexpr int jn = P1Plan::stage(nn < P1Plan::total ? nn : 0), kn = nn - P1Plan::first(jn), cn = P1Plan::order[jn];
        constexpr int j = P1Plan::stage(n < 0 ? 0 : n), k = n - P1Plan::first(j), cs = P1Plan::order[j];
        constexpr bool fetch = nn < P1Plan::total;
        constexpr int ssn = cn < 8 ? (kn >= 4) : kn / 6, un = cn < 8 ? (kn >= 4 ? kn - 4 : kn) : kn % 6, son = (jn % 5) * SLOT;
        constexpr int ss = cs < 8 ? (k >= 4) : k / 6, u = cs < 8 ? (k >= 4 ? k - 4 : k) : k % 6;
        if constexpr (fetch && kn == 0) {
            if constexpr (jn < 15) vm_bar<P1Plan::vm(jn)>();
            if constexpr (jn == 0) { STAMP(1); }
            if constexpr (jn == 4) { STAMP(2); }
            if constexpr (jn == 8) { STAMP(3); }
            if constexpr (jn == 11) { STAMP(4); }
            if constexpr (jn == 14) { STAMP(5); }
            if constexpr (jn == P1Plan::JF) {
                post_tag(ws, WS_F1 + ((2 * cg + nh) * 64 + tile) * 8, tag, lane);
                const f32x2 o = *(const LAS f32x2*)(size_t)(lds0 + LDS_XCH + (w ^ 4) * 512 + lane * 8);
                z0 = LOG2E * (aq[3][0][0] + o[0]);
                z1 = LOG2E * (aq[3][1][0] + o[1]);
            }
#pragma unroll
            for (int q = 0; q < 2; ++q) {
                if constexpr (cn < 8) { bx[q] = xa[q] + son; bq[q] = qa[q] + son; bv[q] = va[q] + son; asm volatile("" : "+v"(bx[q]), "+v"(bq[q]), "+v"(bv[q])); }
                else { bq[q] = fa[q] + son; asm volatile("" : "+v"(bq[q])); }
            }
        }
        unsigned fad = 0;
        if constexpr (fetch) fad = cn < 8 ? (un < 2 ? bq[ssn] + 32 * un : bv[ssn]) : bq[ssn] + (un & 1) * WPART + (un >> 1) * 32;
        auto mul = [&](int m) {
            if constexpr (n >= 0) {
                const bf16x8 wv = cat8(wlo[n & 3], whi[n & 3]), xv = xf[2 * (cs & 7) + ss][m];
                if constexpr (cs >= 8) af[u][m] = MFMA16(wv, xv, af[u][m]);
                else aq[u][m] = MFMA16(wv, xv, aq[u][m]);
            }
        };
        mul(0);
        SCHED();
        if constexpr (fetch) {
            if constexpr (cn < 8 && un == 0) xf[2 * cn + ssn][0] = lds_frag(bx[ssn]);
            if constexpr (cn < 8 && un == 3) {
                const bf16x8 gv = lds_frag(gwa + cn * 128);
                wlo[nn & 3] = __builtin_bit_cast(s16x4, __builtin_shufflevector(gv, gv, 0, 1, 2, 3));
                whi[nn & 3] = __builtin_bit_cast(s16x4, __builtin_shufflevector(gv, gv, 4, 5, 6, 7));
            } else wlo[nn & 3] = TRREAD(fad);
        }
        SCHED();
        mul(1);
        SCHED();
        if constexpr (fetch) {
            if constexpr (cn < 8 && un == 0) xf[2 * cn + ssn][1] = lds_frag(bx[ssn] + 2048);
            if constexpr (!(cn < 8 && un == 3)) whi[nn & 3] = TRREAD(fad + 784);
            constexpr int pc = P1Plan::piece_at(nn);
            if constexpr (pc >= 0) issue(pc / 4, pc % 4);
        }
        SCHED();
        if constexpr (nn == P1Plan::ST) {
            const unsigned qx = lds0 + LDS_Q + w * 1024;
            u32x4 kv[2];
            if (cg < 8) {
#pragma unroll
                for (int e = 0; e < 2; ++e) kv[e] = join16(aq[e][0], aq[e][1]);
            } else {
#pragma unroll
                for (int e = 0; e < 2; ++e) {
#pragma unroll
                    for (int m = 0; m < 2; ++m)
                        *(LAS u32x2*)(size_t)(qx + (16 * m + r16) * 32 + 8 * g4) = (u32x2){pk2(aq[e][m][0], aq[e][m][1]), pk2(aq[e][m][2], aq[e][m][3])};
                    asm volatile("" ::: "memory");
                    const unsigned ra = qx + (8 * g4 + (r16 >> 2)) * 32 + 16 * (lane & 1) + 8 * ((lane >> 1) & 1);
                    kv[e] = __builtin_bit_cast(u32x4, lds_frag_tr(ra, 128));
                    asm volatile("" ::: "memory");
                }
            }
#pragma unroll
            for (int e = 0; e < 2; ++e) st_ws(ws, lane * 16, WS_K + ((4 * cg + 2 * nh + e) * 64 + tile) * 1024, kv[e]);
            *(LAS u32x4*)(size_t)(qx + lane * 16) = join16(aq[2][0] * QSCALE, aq[2][1] * QSCALE);
            *(LAS f32x2*)(size_t)(lds0 + LDS_XCH + w * 512 + lane * 8) = (f32x2){aq[3][0][0], aq[3][1][0]};
            SCHED();
        }
    });
    STAMP(6);
    const int e = nh, kq = mt, head = 2 * cg + e;
    wait_tags(ws, WS_F1 + (((g4 & 1) * 16 + cg) * 64 + kq * 16 + r16) * 8, tag);
    STAMP(7);
    unsigned kb = WS_K + (head * 64 + kq * 16) * 1024;
    const unsigned vo = lane * 16, vv = (WS_V - WS_K) + (lane >> 5) * 256 + r16 * 16;
    u32x16 kv0, kv1, kv2;
    asm volatile(A_CST8(112) A_CST8(124) A_L01 A_CST8(136) A_CST8(148) A_L23
                 : "=&{v[112:127]}"(kv0), "=&{v[128:143]}"(kv1), "=&{v[144:159]}"(kv2), [kb] "+s"(kb)
                 : [vo] "v"(vo), [vv] "v"(vv), [cst] "v"((lane & 27) == 16 ? 0x3F803F80u : 0u), [srd] "s"(sws), [msk] "s"(0x0000FFFF0000FFFFull)
                 : "memory", "scc");
    {
        constexpr float C0 = -2.f * 0.7978845608028654f * LOG2E, C1 = C0 * 0.044715f;
        const float io[2] = {1.f + __builtin_amdgcn_exp2f(fminf(z0, 126.f)), 1.f + __builtin_amdgcn_exp2f(fminf(z1, 126.f))};
#pragma unroll
        for (int u = 0; u < 3; ++u) {
            f32x4 hv[2];
#pragma unroll
            for (int m = 0; m < 2; ++m)
#pragma unroll
                for (int i = 0; i < 4; i += 2) {
                    const f32x2 t = {af[2 * u][m][i], af[2 * u][m][i + 1]}, up = {af[2 * u + 1][m][i], af[2 * u + 1][m][i + 1]};
                    const f32x2 a = t * __builtin_elementwise_fma(t * t, (f32x2){C1, C1}, (f32x2){C0, C0});
                    const f32x2 ex = {__builtin_amdgcn_exp2f(a[0]), __builtin_amdgcn_exp2f(a[1])};
                    const f32x2 d = __builtin_elementwise_fma(ex, (f32x2){io[m], io[m]}, (f32x2){io[m], io[m]});
                    const f32x2 hh = (t * up) * (f32x2){__builtin_amdgcn_rcpf(d[0]), __builtin_amdgcn_rcpf(d[1])};
                    hv[m][i] = hh[0]; hv[m][i + 1] = hh[1];
                }
            const int unit = ustart + 3 * nh + u;
            st_ws(ws, (g4 & 1) * NKS * 1024 + (g4 >> 1) * 256 + r16 * 16,
                  3 * nh + u < nunit ? WS_A + (2 * tile * NKS + (unit >> 1)) * 1024 + (unit & 1) * 512 : 0x7ff00000u, join16(hv[0], hv[1]));
        }
    }
    float g = __builtin_amdgcn_rcpf(1.f + __builtin_amdgcn_exp2f(-((g4 & 1) ? z1 : z0)));
    asm volatile("" : "+v"(g));
    bf16x8 qf[4];
#pragma unroll
    for (int i = 0; i < 4; ++i) qf[i] = lds_frag(lds0 + LDS_Q + (e * 4 + ((i + kq) & 3)) * 1024 + lane * 16);
    STAMP(8);
    LGKM_BAR();
    const int m3 = w & 1, k3 = w >> 1;
    const u32x4 sd = make_srd(pwd, FF * DM * 2), so = make_srd(pwo, DM * DM * 2);
    const unsigned vw0 = (16 * w + (lane >> 3)) * 1024 + cb * 128 + (((lane & 7) ^ ((g4 & 1) << 1)) << 4), vw1 = (vw0 ^ 64) + 8192;
    const unsigned ab = WS_A + ((rt * 4 + m3 * 2) * NKS + k3) * 1024;
    f32x16 o[4] = {};
    u32x16 h0, h1, h2, h3, h4;
    {
        unsigned wl = lds0 + w * 2048, wo = 0, hb = ab, cap = SPIN_CAP;
        u64 tmp;
        asm volatile(
            "v_mov_b32 v160, 0xdddddddd\n\t"
            "v_mov_b32 v88, 0\n\tv_mov_b32 v89, 0\n\tv_mov_b32 v90, 0\n\tv_mov_b32 v91, 0\n\t"
            "v_mov_b32 v92, 0\n\tv_mov_b32 v93, 0\n\tv_mov_b32 v94, 0\n\tv_mov_b32 v95, 0\n\t"
            A_W(9)
            "v_mfma_f32_32x32x16_bf16 v[64:79], v[112:115], v[96:99], 0\n\t"
            "s_nop 11\n\t"
            ".p2align 3\n\t"
            A_KEYT(112, 116, 120, 120, 124, "", "", "", "") A_T1("", "", "", A_W(3))
            A_T2(A_L01, A_D0, A_D1) A_T3(A_D0, A_D1, "", A_W(4))
            A_T0(A_L23, A_POST A_D0, A_D1) A_T1(A_D0, A_D1, "", A_W(5))
            A_T2(A_L01 A_POLL, A_D0, A_D1) A_T3(A_D0, A_D1, "", A_W(4) A_CHECK)
            A_T0(A_L23, A_H(164), "") A_T1(A_H(172), A_H(180), "", A_W(6))
            A_T2(A_L01, A_H(188), "") A_T3(A_H(196), A_H(204), "", A_W(6))
            A_T0(A_L23, A_H(212), "") A_T1(A_H(220), A_H(228), "", A_W(6))
            A_T2(A_H(236), "", "") A_T3("", "", "", "")
            "s_waitcnt vmcnt(0)\n\ts_nop 0\n\t"
            "v_smfmac_f32_32x32x32_bf16 v[48:63], v[156:159], v[88:95], v160\n\t"
            "s_nop 15"
            : "+{v[0:15]}"(o[0]), "+{v[16:31]}"(o[1]), "+{v[32:47]}"(o[2]), "+{v[48:63]}"(o[3]),
              "+{v[112:127]}"(kv0), "+{v[128:143]}"(kv1), "+{v[144:159]}"(kv2),
              "=&{v[164:179]}"(h0), "=&{v[180:195]}"(h1), "=&{v[196:211]}"(h2), "=&{v[212:227]}"(h3), "=&{v[228:243]}"(h4),
              [kb] "+s"(kb), [wl] "+s"(wl), [wo] "+s"(wo), [hb] "+s"(hb), [cap] "+s"(cap), [tmp] "=&s"(tmp)
            : "{v[96:111]}"(__builtin_bit_cast(u32x16, qf)), [vo] "v"(vo), [vv] "v"(vv), [vw0] "v"(vw0), [vw1] "v"(vw1),
              [srd] "s"(sws), [sd] "s"(sd), [msk] "s"(0x0000FFFF0000FFFFull), [tg0] "s"((unsigned)tag), [tg1] "s"((unsigned)(tag >> 32)),
              [fh] "s"(WS_FH + (tile * 32 + 2 * cg + nh) * 8), [fp] "s"(WS_FH + (rt * 2 + m3) * 256)
            : "memory", "scc", "vcc", "v64", "v65", "v66", "v67", "v68", "v69", "v70", "v71", "v72", "v73", "v74", "v75", "v76", "v77", "v78", "v79",
              "v80", "v81", "v82", "v83", "v84", "v85", "v86", "v87", "v88", "v89", "v90", "v91", "v92", "v93", "v94", "v95",
              "v160", "v161", "v162", "v163");
    }
    STAMP(9);
    asm volatile("" : "+v"(lane));
    r16 = lane & 15; g4 = lane >> 4;
    bf16x8 a3[15][2];
#pragma unroll
    for (int m = 0; m < 2; ++m) a3[10][m] = ld_ws(ws, k3 == 3 ? 0x7ff00000u : vo, ab + (m * NKS + 40) * 1024);
    SCHED();
    f32x8 oo;
    float l = attn_reduce(lds0 + LDS_RED, o, oo, e, kq, lane);
    {
        float am = 0.f;
#pragma unroll
        for (int j = 0; j < 8; ++j) am = fmaxf(am, fabsf(oo[j]));
        const bool ok = l >= 0x1p-40f && l <= 0x1p40f && am <= 0x1p120f;
        const int bad = __any(!ok);
        if (lane == 0) *(LAS int*)(size_t)(lds0 + LDS_FLG + w * 4) = bad;
        LGKM_BAR();
        const u32x4 f0 = *(const LAS u32x4*)(size_t)(lds0 + LDS_FLG), f1 = *(const LAS u32x4*)(size_t)(lds0 + LDS_FLG + 16);
        if (__builtin_expect(__builtin_amdgcn_readfirstlane(f0[0] | f0[1] | f0[2] | f0[3] | f1[0] | f1[1] | f1[2] | f1[3]), 0)) {
            const f32x16 zero = {};
            float mx[4] = {-INFINITY, -INFINITY, -INFINITY, -INFINITY};
#pragma unroll 1
            for (int t = 0; t < 64; ++t) {
                const bf16x8 k0 = ld_ws(ws, lane * 16, WS_K + (head * 64 + t) * 1024);
#pragma unroll
                for (int i = 0; i < 4; ++i) {
                    const f32x16 s = MFMA(k0, qf[i], zero);
#pragma unroll
                    for (int j = 0; j < 16; ++j) mx[i] = fmaxf(mx[i], s[j]);
                }
            }
#pragma unroll
            for (int i = 0; i < 4; ++i) { mx[i] = half_max(mx[i]); o[i] = zero; }
            const unsigned k0b = WS_K + (head * 64 + kq * 16) * 1024;
            attn_keys_safe(ws, qf, mx, o, k0b, k0b + (WS_V - WS_K), lane);
            l = attn_reduce(lds0 + LDS_RED, o, oo, e, kq, lane);
            LGKM_BAR();
        }
        const float sc = g * __builtin_amdgcn_rcpf(l);
        st_ws(ws, (g4 & 1) * NKS * 1024 + (g4 >> 1) * 256 + r16 * 16, WS_A + (2 * tile * NKS + 43 + cg) * 1024 + e * 512,
              (u32x4){pk2(oo[0] * sc, oo[1] * sc), pk2(oo[2] * sc, oo[3] * sc), pk2(oo[4] * sc, oo[5] * sc), pk2(oo[6] * sc, oo[7] * sc)});
    }
    STAMP(10);
    {
        const u32x16 hh[5] = {h0, h1, h2, h3, h4};
#pragma unroll
        for (int st = 0; st < 10; ++st)
#pragma unroll
            for (int m = 0; m < 2; ++m) {
                const int q = (st & 1) * 8 + m * 4;
                a3[st][m] = __builtin_bit_cast(bf16x8, (u32x4){hh[st >> 1][q], hh[st >> 1][q + 1], hh[st >> 1][q + 2], hh[st >> 1][q + 3]});
            }
    }
    const unsigned wb3 = lds0 + (32 * k3 + 8 * g4 + (r16 >> 2)) * 128 + (((r16 >> 3) | ((g4 & 1) << 1)) << 5) + 8 * (lane & 3);
    f32x4 acc[4][2] = {};
    unsigned b3[4];
    static_for<-3, 60>([&](auto I) {
        constexpr int n = I.v, nn = n + 3, stn = nn / 4, nun = nn % 4, st = n / 4, nu = n % 4;
        constexpr bool fetch = nn < 60;
        if constexpr (nn == 16 || nn == 44) asm volatile("s_barrier" ::: "memory");
        if constexpr (nn == 24) {
            vm_bar<8>();
            STAMP(11);
            post_tag(ws, WS_FO + (tile * 32 + head) * 8, tag, lane);
        }
        if constexpr (nn == 36) {
            STAMP(12);
            wait_tags(ws, WS_FO + ((rt * 2 + m3) * 32 + ((lane >> 1) & 3) * 8 + 2 * k3 + (lane & 1)) * 8, tag);
            STAMP(13);
#pragma unroll
            for (int s2 = 11; s2 < 15; ++s2)
#pragma unroll
                for (int m = 0; m < 2; ++m) a3[s2][m] = ld_ws(ws, lane * 16, ab + (m * NKS + s2 * 4 - 1) * 1024);
            vm_bar<8>();
        }
        constexpr int slot = stn % 10;
        if constexpr (fetch && nun == 0 && slot % 4 == 0) {
#pragma unroll
            for (int q = 0; q < 4; ++q) { b3[q] = (wb3 ^ (q * 32)) + slot * 16384; asm volatile("" : "+v"(b3[q])); }
        }
        const unsigned fad = b3[nun] + (slot & 3) * 16384;
        if constexpr (n >= 0) acc[nu][0] = MFMA16(cat8(wlo[n & 3], whi[n & 3]), a3[st][0], acc[nu][0]);
        SCHED();
        if constexpr (fetch) wlo[nn & 3] = TRREAD(fad);
        SCHED();
        if constexpr (n >= 0) acc[nu][1] = MFMA16(cat8(wlo[n & 3], whi[n & 3]), a3[st][1], acc[nu][1]);
        SCHED();
        if constexpr (fetch) whi[nn & 3] = TRREAD(fad + 512);
        constexpr int pc = nn < 12 ? (nn % 2 == 0 ? 12 + nn / 2 : -1) : nn < 16 ? -1 : nn < 24 ? 18 + nn - 16 : (nn < 32 && nn % 2 == 0) ? 26 + (nn - 24) / 2 : -1;
        if constexpr (pc >= 0) {
            constexpr int s2 = pc / 2, i = pc & 1;
            dma16(s2 < 11 ? sd : so, i ? vw1 : vw0, (s2 < 11 ? s2 : s2 - 11) * 131072, lds0 + (s2 % 10) * 16384 + w * 2048 + i * 1024);
        }
        SCHED();
    });
    STAMP(14);
    const unsigned red3 = lds0 + 5 * 16384;
#pragma unroll
    for (int nu = 0; nu < 4; ++nu)
#pragma unroll
        for (int m = 0; m < 2; ++m)
            *(LAS f32x4*)(size_t)(red3 + (k3 * 64 + m3 * 32 + m * 16 + r16) * 272 + (nu * 16 + 4 * g4) * 4) = acc[nu][m];
    LGKM_BAR();
    const int row = w * 8 + (lane >> 3), c8 = lane & 7;
    f32x4 lo = {}, hi = {};
#pragma unroll
    for (int q = 0; q < 4; ++q) {
        const unsigned ad = red3 + (q * 64 + row) * 272 + c8 * 32;
        lo += *(const LAS f32x4*)(size_t)ad;
        hi += *(const LAS f32x4*)(size_t)(ad + 16);
    }
    *(u32x4*)((char*)pout + (rt * 64 + row) * 1024 + cb * 128 + c8 * 16) = (u32x4){pk2(lo[0], lo[1]), pk2(lo[2], lo[3]), pk2(hi[0], hi[1]), pk2(hi[2], hi[3])};
    if (b == 0 && w == 0) post_tag(ws, WS_EP, tag, lane);
}

extern "C" void kernel_launch(void* const* d_in, const int* in_sizes, int n_in, void* d_out, int out_size, void* d_ws, size_t ws_size,
                              hipStream_t stream) {
    (void)hipFuncSetAttribute((const void*)gm_kernel, hipFuncAttributeMaxDynamicSharedMemorySize, LDS_BYTES);
    gm_kernel<<<256, 512, LDS_BYTES, stream>>>(d_in[0], d_in[1], d_in[2], d_in[4], d_in[5], d_in[6], d_in[7], d_out, d_ws);
}
```

```cpp
#define WARM_IN
#include <hip/hip_runtime.h>
#include <hip/hip_bf16.h>

typedef float f32x2 __attribute__((ext_vector_type(2)));
typedef float f32x4 __attribute__((ext_vector_type(4)));
typedef float f32x8 __attribute__((ext_vector_type(8)));
typedef float f32x16 __attribute__((ext_vector_type(16)));
typedef __bf16 bf16x2 __attribute__((ext_vector_type(2)));
typedef __bf16 bf16x8 __attribute__((ext_vector_type(8)));
typedef __bf16 bf16x16 __attribute__((ext_vector_type(16)));
typedef short s16x4 __attribute__((ext_vector_type(4)));
typedef unsigned u32x2 __attribute__((ext_vector_type(2)));
typedef unsigned u32x4 __attribute__((ext_vector_type(4)));
#define LAS __attribute__((address_space(3)))
#define DI __device__ __forceinline__

constexpr int SEQ = 2048, DM = 512, FF = 1376;
constexpr int NKS = 59;
constexpr unsigned WS_K = 0, WS_V = 2u << 20, WS_A = 4u << 20, WS_F1 = 12u << 20, WS_FH = WS_F1 + 2048 * 128,
                   WS_FO = WS_FH + 2048 * 128, WS_EP = WS_FO + 2048 * 128, WS_END = WS_EP + 256;
constexpr int WPART = 16 * 784;
constexpr int SLOT = 16384 + WPART, LDS_XCH = 5 * SLOT, LDS_GW = LDS_XCH + 8 * 512, LDS_DUMMY = LDS_GW + 1024, LDS_Q = LDS_DUMMY + 256;
constexpr int HS = 8192;
constexpr int LDS_RED = 13 * HS, LDS_SUM = 10 * HS, LDS_BYTES = 160 * 1024;
static_assert(LDS_Q + 8192 <= LDS_BYTES && LDS_RED + 24 * 2304 <= LDS_BYTES, "LDS map");
constexpr float LOG2E = 1.4426950408889634f, QSCALE = 0.25f * LOG2E;
constexpr int SPIN_CAP = 1 << 16;
typedef unsigned long long u64;

DI u32x4 make_srd(const void* p, unsigned bytes) {
    const unsigned long long a = (unsigned long long)p;
    return (u32x4){(unsigned)a, (unsigned)(a >> 32) & 0xffffu, bytes, 0x00020000u};
}
DI void dma16(u32x4 srd, unsigned voff, unsigned soff, unsigned lds) {
    asm volatile("s_mov_b32 m0, %0\n\ts_nop 0\n\tbuffer_load_dwordx4 %1, %2, %3 offen lds" ::"s"(lds), "v"(voff), "s"(srd), "s"(soff) : "memory");
}
template <int N> DI void vm_bar() { asm volatile("s_waitcnt vmcnt(%0)\n\ts_barrier" ::"i"(N) : "memory"); }
#define SCHED() __builtin_amdgcn_sched_barrier(0)
template <int I> struct IC { static constexpr int v = I; };
template <int B, int E, class Fn> DI void static_for(Fn&& f) { if constexpr (B < E) { f(IC<B>{}); static_for<B + 1, E>(f); } }
#define BAR() asm volatile("s_barrier" ::: "memory")
#define LGKM_BAR() asm volatile("s_waitcnt lgkmcnt(0)\n\ts_barrier" ::: "memory")
template <int N> DI void vm_wait() { asm volatile("s_waitcnt vmcnt(%0)" ::"i"(N) : "memory"); }
template <bool FIRST> DI void mma(f32x4& acc, bf16x8 a, bf16x8 b) {
    if constexpr (FIRST) asm volatile("v_mfma_f32_16x16x32_bf16 %0, %1, %2, 0" : "=&v"(acc) : "v"(a), "v"(b));
    else asm volatile("v_mfma_f32_16x16x32_bf16 %0, %1, %2, %0" : "+v"(acc) : "v"(a), "v"(b));
}
DI void mfma_done() { asm volatile("s_nop 7" ::: "memory"); SCHED(); }
DI float mul1(float a, float b) { float r; asm("v_mul_f32 %0, %1, %2" : "=v"(r) : "v"(a), "v"(b)); return r; }

#if __HIP_DEVICE_COMPILE__
#define MFMA(a, b, c) __builtin_amdgcn_mfma_f32_32x32x16_bf16((a), (b), (c), 0, 0, 0)
#define SMFMAC(a, b, c) __builtin_amdgcn_smfmac_f32_32x32x32_bf16((a), __builtin_bit_cast(bf16x16, (b)), (c), (int)0xDDDDDDDD, 0, 0)
#define MFMA16K16(a, b, c) __builtin_amdgcn_mfma_f32_16x16x16bf16_1k((a), (b), (c), 0, 0, 0)
#define TRREAD(a) __builtin_amdgcn_ds_read_tr16_b64_v4i16((LAS s16x4*)(size_t)(a))
#else
#define MFMA(a, b, c) (c)
#define SMFMAC(a, b, c) (c)
#define MFMA16K16(a, b, c) (c)
#define TRREAD(a) (s16x4{})
#endif
DI bf16x8 lds_frag(unsigned a) { return *(const LAS bf16x8*)(size_t)a; }
DI unsigned pk2(float a, float b) { return __builtin_bit_cast(unsigned, __builtin_convertvector((f32x2){a, b}, bf16x2)); }
DI bf16x8 cat8(s16x4 lo, s16x4 hi) { return __builtin_bit_cast(bf16x8, __builtin_shufflevector(lo, hi, 0, 1, 2, 3, 4, 5, 6, 7)); }
DI u32x4 join16(f32x4 a, f32x4 b) {
    auto r0 = __builtin_amdgcn_permlane16_swap(pk2(a[0], a[1]), pk2(b[0], b[1]), false, false);
    auto r1 = __builtin_amdgcn_permlane16_swap(pk2(a[2], a[3]), pk2(b[2], b[3]), false, false);
    return (u32x4){r0[0], r1[0], r0[1], r1[1]};
}
DI float half_max(float v) {
    auto r = __builtin_amdgcn_permlane32_swap(__float_as_uint(v), __float_as_uint(v), false, false);
    return fmaxf(__uint_as_float(r[0]), __uint_as_float(r[1]));
}
typedef __amdgpu_buffer_rsrc_t rsrc_t;
DI rsrc_t make_rsrc(const void* p, unsigned bytes) { return __builtin_amdgcn_make_buffer_rsrc((void*)p, 0, bytes, 0x00020000); }
constexpr int AUX_DEV = 16;
DI void st_ws(rsrc_t ws, unsigned voff, unsigned soff, u32x4 v) { __builtin_amdgcn_raw_buffer_store_b128(v, ws, voff + soff, 0, AUX_DEV); }
DI bf16x8 ld_ws(rsrc_t ws, unsigned voff, unsigned soff) { return __builtin_bit_cast(bf16x8, __builtin_amdgcn_raw_buffer_load_b128(ws, voff, soff, AUX_DEV)); }

struct Tag { unsigned lo, hi; };
DI void post_tag(rsrc_t ws, unsigned off, Tag tag, int lane) {
    if (lane == 0) __builtin_amdgcn_raw_buffer_store_b64((u32x2){tag.lo, tag.hi}, ws, off, 0, AUX_DEV);
}
DI u32x2 peek_tags(u32x4 sws, unsigned voff) {
    u32x2 v;
    asm volatile("buffer_load_dwordx2 %0, %1, %2, 0 offen sc1" : "=&v"(v) : "v"(voff), "s"(sws) : "memory");
    return v;
}
DI void wait_tags(rsrc_t ws, unsigned voff, Tag tag) {
#pragma unroll 1
    for (int spin = 0; spin < SPIN_CAP; ++spin) {
        asm volatile("" ::: "memory");
        const u32x2 v = __builtin_amdgcn_raw_buffer_load_b64(ws, voff, 0, AUX_DEV);
        if (__all(v[0] == tag.lo && v[1] == tag.hi)) break;
        __builtin_amdgcn_s_sleep(1);
    }
    asm volatile("" ::: "memory");
}
DI void land_tags(u32x2& v) { asm volatile("s_waitcnt vmcnt(0)" : "+v"(v) :: "memory"); }
DI void check_tags(rsrc_t ws, unsigned voff, Tag tag, u32x2 v) {
    if (__builtin_expect(!__all(v[0] == tag.lo && v[1] == tag.hi), 0)) wait_tags(ws, voff, tag);
}

#define STAMP(i)

#define CODEPF_WAVE 7
#define P1_ORDER 0, 1, 2, 3, 4, 5, 6, 7, 8, 9, 10, 11, 12, 13, 14, 15
#define P1_PD 4
#define P1_JF 2
namespace P1Plan {
    constexpr int order[16] = {P1_ORDER};
    constexpr int PD = P1_PD;
    constexpr int cnt(int j) { return order[j] < 8 ? 7 : 12; }
    constexpr int first(int j) { int n = 0; for (int i = 0; i < j; ++i) n += cnt(i); return n; }
    constexpr int stage(int n) { int j = 0; while (j < 15 && first(j + 1) <= n) ++j; return j; }
    constexpr int pos(int c) { int j = 0; while (order[j] != c) ++j; return j; }
    constexpr int total = 8 * 7 + 8 * 12;
    constexpr int PRO = 3;
    constexpr int need(int j) { return j == 14 ? 15 : j; }
    constexpr int JS = pos(7) + 2;
    constexpr int JF = JS + P1_JF;
    constexpr int issued(int j) { return j + 2 < 15 ? j + 2 : 15; }
    constexpr int vm(int j, int nh) {
        const int js = JS + nh, before = issued(js);
        int n = 4 * (issued(j) - need(j));
        if (j == JF) { const int y = 4 * (issued(j) - before); return n < y ? n : y; }
        if (j >= js && j < JF && need(j) <= before) n += 2;
        if (j > JF && need(j) <= issued(JF)) n += 1;
        return n;
    }
}
static_assert(P1Plan::JF > P1Plan::JS && P1Plan::JF < 14, "P1 plan");
template <int SL, int C>
DI void p1_issue_a(unsigned lx, unsigned lw, unsigned vx0, unsigned vx1, unsigned vwq, u32x4 sx, u32x4 sq, u64 mk) {
    unsigned t;
    asm volatile("s_add_u32 m0, %[lx], %[a0]\n\ts_mov_b32 %[t], %[k0]\n\tbuffer_load_dwordx4 %[v0], %[s0], %[t] offen lds\n\t"
                 "s_add_u32 m0, %[lx], %[a0]+1024\n\ts_nop 0\n\tbuffer_load_dwordx4 %[v1], %[s0], %[t] offen lds\n\t"
                 "s_add_u32 m0, %[lw], %[a0]+16384\n\ts_mov_b32 %[t], %[k1]\n\ts_mov_b64 exec, %[mk]\n\tbuffer_load_dwordx4 %[v2], %[s1], %[t] offen lds\n\t"
                 "s_add_u32 m0, %[lw], %[a0]+16384+784\n\ts_mov_b32 %[t], %[k1]+4*3072\n\tbuffer_load_dwordx4 %[v2], %[s1], %[t] offen lds\n\t"
                 "s_mov_b64 exec, -1"
                 : [t] "=&s"(t)
                 : [lx] "s"(lx), [lw] "s"(lw), [a0] "i"(SL), [k0] "i"(C * 128), [k1] "i"(C * 64 * 3072),
                   [v0] "v"(vx0), [v1] "v"(vx1), [v2] "v"(vwq), [s0] "s"(sx), [s1] "s"(sq), [mk] "s"(mk)
                 : "memory", "scc");
}
template <int SL, int C>
DI void p1_issue_b(unsigned lw, unsigned vwf, u32x4 sg, u32x4 su, u64 mk) {
    unsigned t;
    asm volatile("s_add_u32 m0, %[lw], %[a0]\n\ts_mov_b32 %[t], %[k0]\n\ts_mov_b64 exec, %[mk]\n\tbuffer_load_dwordx4 %[v0], %[s0], %[t] offen lds\n\t"
                 "s_add_u32 m0, %[lw], %[a0]+%[wp]\n\ts_nop 0\n\tbuffer_load_dwordx4 %[v0], %[s1], %[t] offen lds\n\t"
                 "s_add_u32 m0, %[lw], %[a0]+784\n\ts_mov_b32 %[t], %[k0]+4*2752\n\tbuffer_load_dwordx4 %[v0], %[s0], %[t] offen lds\n\t"
                 "s_add_u32 m0, %[lw], %[a0]+%[wp]+784\n\ts_nop 0\n\tbuffer_load_dwordx4 %[v0], %[s1], %[t] offen lds\n\t"
                 "s_mov_b64 exec, -1"
                 : [t] "=&s"(t)
                 : [lw] "s"(lw), [a0] "i"(SL), [wp] "i"(WPART), [k0] "i"(C * 64 * 2752), [v0] "v"(vwf), [s0] "s"(sg), [s1] "s"(su), [mk] "s"(mk)
                 : "memory", "scc");
}
DI void opaque(unsigned& a) { asm volatile("" : "+v"(a)); }

DI float attn_safe(rsrc_t ws, bf16x8 q, f32x8& oo, unsigned kb, int lane) {
    const f32x16 zero = {};
    float mx = -INFINITY;
#pragma unroll 1
    for (int t = 0; t < 64; ++t) {
        const f32x16 s = MFMA(ld_ws(ws, lane * 16, kb + t * 1024), q, zero);
#pragma unroll
        for (int j = 0; j < 16; ++j) mx = fmaxf(mx, s[j]);
    }
    mx = half_max(mx);
    const unsigned cst = (lane & 11) == 0 ? 0x3F803F80u : 0u, vv = (WS_V - WS_K) + (lane >> 5) * 256 + (lane & 15) * 16;
    const bf16x8 cf = __builtin_bit_cast(bf16x8, (u32x4){cst, cst, cst, cst});
    f32x16 o = zero;
#pragma unroll 1
    for (int t = 0; t < 64; ++t) {
        f32x16 s = MFMA(ld_ws(ws, lane * 16, kb + t * 1024), q, zero);
        bf16x8 va = ld_ws(ws, vv, kb + t * 1024), vc = ld_ws(ws, vv, kb + t * 1024 + 512);
        if (lane & 16) { va = cf; vc = cf; }
#pragma unroll
        for (int j = 0; j < 16; ++j) s[j] = __builtin_amdgcn_exp2f(s[j] - mx);
        o = SMFMAC(va, __builtin_shufflevector(s, s, 0, 1, 2, 3, 4, 5, 6, 7), o);
        o = SMFMAC(vc, __builtin_shufflevector(s, s, 8, 9, 10, 11, 12, 13, 14, 15), o);
    }
    oo = __builtin_shufflevector(o, o, 0, 1, 2, 3, 4, 5, 6, 7);
    return o[8];
}
#define A_EXP(S, i) "v_exp_f32 v[" #S "+" #i "], v[" #S "+" #i "]\n\t"
#define A_TILE(S, T, OP, VBP, KN, QN, OC, VAC, EXTRA, WAIT)                                              \
    A_EXP(S, 0) A_EXP(S, 1)                                                                              \
    "v_smfmac_f32_32x32x32_bf16 v[" #OP ":" #OP "+15], v[" #VBP ":" #VBP "+3], v[" #T "+8:" #T "+15], v160\n\t" \
    EXTRA A_EXP(S, 2) A_EXP(S, 3) A_EXP(S, 4) WAIT                                                       \
    "v_mfma_f32_32x32x16_bf16 v[" #T ":" #T "+15], v[" #KN ":" #KN "+3], v[" #QN ":" #QN "+3], 0\n\t"    \
    A_EXP(S, 5) A_EXP(S, 6) A_EXP(S, 7) A_EXP(S, 8) A_EXP(S, 9)                                          \
    "v_smfmac_f32_32x32x32_bf16 v[" #OC ":" #OC "+15], v[" #VAC ":" #VAC "+3], v[" #S ":" #S "+7], v160\n\t" \
    A_EXP(S, 10) A_EXP(S, 11) A_EXP(S, 12) A_EXP(S, 13) A_EXP(S, 14) A_EXP(S, 15)
#define A_KEYT(KC, VA, VB, VBP, KN, E0, E1, E2, E3, WAIT)   \
    A_TILE(64, 80, 48, VBP, KC, 100, 0, VA, E0, "")         \
    A_TILE(80, 64, 0, VB, KC, 104, 16, VA, E1, "")          \
    A_TILE(64, 80, 16, VB, KC, 108, 32, VA, E2, "")         \
    A_TILE(80, 64, 32, VB, KN, 96, 48, VA, E3, WAIT)
#define A_T0(E0, E1, E2, W) A_KEYT(112, 116, 120, 156, 124, E0, E1, E2, "", W)
#define A_T1(E0, E1, E2, W) A_KEYT(124, 128, 132, 120, 136, E0, E1, E2, "", W)
#define A_T2(E0, E1, E2) A_KEYT(136, 140, 144, 132, 148, E0, E1, E2, "", "")
#define A_T3(E0, E1, E2, W) A_KEYT(148, 152, 156, 144, 112, E0, E1, E2, "", W)
#define A_LOAD2(R0, R1)                                                                            \
    "buffer_load_dwordx4 v[" #R0 ":" #R0 "+3], %[vo], %[srd], %[kb] offen sc1\n\t"                 \
    "buffer_load_dwordx4 v[" #R1 ":" #R1 "+3], %[vo], %[srd], %[kb] offen offset:1024 sc1\n\t"     \
    "s_mov_b64 exec, %[msk]\n\t"                                                                   \
    "buffer_load_dwordx4 v[" #R0 "+4:" #R0 "+7], %[vv], %[srd], %[kb] offen sc1\n\t"               \
    "buffer_load_dwordx4 v[" #R0 "+8:" #R0 "+11], %[vv], %[srd], %[kb] offen offset:512 sc1\n\t"   \
    "buffer_load_dwordx4 v[" #R1 "+4:" #R1 "+7], %[vv], %[srd], %[kb] offen offset:1024 sc1\n\t"   \
    "buffer_load_dwordx4 v[" #R1 "+8:" #R1 "+11], %[vv], %[srd], %[kb] offen offset:1536 sc1\n\t"  \
    "s_mov_b64 exec, -1\n\t"                                                                       \
    "s_add_u32 %[kb], %[kb], 2048\n\t"
#define A_L01 A_LOAD2(112, 124)
#define A_L23 A_LOAD2(136, 148)
#define A_CST8(R) "v_mov_b64 v[" #R "+4:" #R "+5], %[cst]\n\tv_mov_b64 v[" #R "+6:" #R "+7], %[cst]\n\t" \
                  "v_mov_b64 v[" #R "+8:" #R "+9], %[cst]\n\tv_mov_b64 v[" #R "+10:" #R "+11], %[cst]\n\t"
#define A_W(N) "s_waitcnt vmcnt(" #N ")\n\t"
#define A_P(N) "s_setprio " #N "\n\t"
#define A_D "s_mov_b32 m0, %[wl]\n\ts_add_u32 %[wl], %[wl], 8192\n\tbuffer_load_dwordx4 %[vw], %[sd], %[wo] offen lds\n\ts_add_u32 %[wo], %[wo], 65536\n\t"
#define A_WARM "v_lshlrev_b32 v161, 6, %[vo]\n\ts_mov_b64 exec, 0x1ffff\n\tbuffer_load_dword v162, v161, %[sd], %[w1] offen\n\t" \
               "s_mov_b64 exec, 0xffff\n\tbuffer_load_dword v163, v161, %[so], %[w2] offen\n\ts_mov_b64 exec, -1\n\t"
#define A_H(R) "buffer_load_dwordx4 v[" #R ":" #R "+3], %[vo], %[srd], %[hb] offen sc1\n\ts_add_u32 %[hb], %[hb], 60416\n\t" \
               "buffer_load_dwordx4 v[" #R "+4:" #R "+7], %[vo], %[srd], %[hb] offen sc1\n\ts_sub_u32 %[hb], %[hb], 58368\n\t"
static_assert(NKS * 1024 == 60416 && 60416 - 2048 == 58368, "A_H strides");
#define A_R(O, RA) "v_add_u32 v161, %[" #RA "], %[vo]\n\tv_lshrrev_b32 v162, 2, %[vo]\n\tv_add_u32 v162, %[" #RA "], v162\n\t" \
                   "ds_write_b128 v161, v[" #O ":" #O "+3]\n\tds_write_b128 v161, v[" #O "+4:" #O "+7] offset:1024\n\tds_write_b32 v162, v[" #O "+8] offset:2048\n\t"
#define A_Z(O) "v_mov_b64 v[" #O ":" #O "+1], 0\n\tv_mov_b64 v[" #O "+2:" #O "+3], 0\n\tv_mov_b64 v[" #O "+4:" #O "+5], 0\n\tv_mov_b64 v[" #O "+6:" #O "+7], 0\n\t"
#define A_POST "v_mov_b32 v161, %[fh]\n\tv_mov_b32 v162, %[tg0]\n\tv_mov_b32 v163, %[tg1]\n\ts_mov_b64 exec, 1\n\t" \
               "buffer_store_dwordx2 v[162:163], v161, %[srd], 0 offen sc1\n\ts_mov_b64 exec, -1\n\t"
#define A_POLL "v_and_b32 v161, 0x3e0, %[vo]\n\tv_lshlrev_b32 v161, 2, v161\n\tbuffer_load_dwordx2 v[162:163], v161, %[srd], %[fp] offen sc1\n\t"
#define A_CHECK "2:\n\tv_cmp_ne_u32 vcc, %[tg0], v162\n\tv_cmp_ne_u32 %[tmp], %[tg1], v163\n\ts_or_b64 vcc, vcc, %[tmp]\n\ts_cbranch_scc0 3f\n\t" \
                "s_sub_u32 %[cap], %[cap], 1\n\ts_cbranch_scc1 3f\n\ts_sleep 1\n\t"                                                             \
                "buffer_load_dwordx2 v[162:163], v161, %[srd], %[fp] offen sc1\n\ts_waitcnt vmcnt(0)\n\ts_branch 2b\n3:\n\t"
#define A_S(i) ""
#define A_SOUT
typedef unsigned u32x16 __attribute__((ext_vector_type(16)));
typedef unsigned u32x8 __attribute__((ext_vector_type(8)));

__global__ __launch_bounds__(512, 2) void gm_kernel(const void* px, const void* pwqkv, const void* pwg, const void* pwu, const void* pgw,
                                                    void* pws, const void* pwd, const void* pwo, void* pout) {
    extern __shared__ __attribute__((aligned(16))) char smem[];
    const unsigned lds0 = (unsigned)(size_t)(LAS char*)smem;
    int lane = threadIdx.x & 63;
    const int w = __builtin_amdgcn_readfirstlane(threadIdx.x >> 6);
    const int b = blockIdx.x, xcd = b & 7, jb = b >> 3;
    const int rb = (xcd >> 2) * 8 + (jb & 7), cg = (xcd & 3) * 4 + (jb >> 3);
    const int rt = xcd * 4 + (jb >> 3), cb = jb & 7;
    STAMP(0);
    {
        u32x4 sc;
        unsigned lo, hi, len;
        asm volatile("s_getpc_b64 vcc\n.Lgm_code_lo:\n\ts_mov_b32 %0, vcc_lo\n\ts_and_b32 %1, vcc_hi, 0xffff\n\ts_mov_b32 %2, .Lgm_code_hi-.Lgm_code_lo"
                     : "=s"(lo), "=s"(hi), "=s"(len) :: "vcc", "scc");
        sc = (u32x4){lo, hi, len, 0x00020000u};
        if (w == CODEPF_WAVE)
            asm volatile("s_mov_b32 m0, %0\n\ts_nop 0\n\tbuffer_load_dword %1, %2, 0 offen lds" ::"s"(lds0 + LDS_DUMMY), "v"(((jb & 7) * 64 + lane) * 128), "s"(sc) : "memory");
    }
    int r16 = lane & 15, g4 = lane >> 4;
    const rsrc_t ws = make_rsrc(pws, WS_END);
    const u32x4 sws = make_srd(pws, WS_END);
    const int mt = w & 3, nh = w >> 2, tile = rb * 4 + mt;
    const u32x4 sx = make_srd(px, SEQ * DM * 2), sq = make_srd(pwqkv, DM * 3 * DM * 2);
    const u32x4 sg = make_srd(pwg, DM * FF * 2), su = make_srd(pwu, DM * FF * 2), sw = make_srd(pgw, DM * 2);
    {
        const int per = mt == 0 ? 32 : mt == 1 ? 24 : 22;
        const u32x4 sr = mt == 0 ? sx : mt == 1 ? sq : mt == 2 ? sg : su;
        const unsigned a = lane < per ? ((2 * b + nh) * per + lane) * 128 : 0x7fffff00u;
        asm volatile("s_mov_b32 m0, %0\n\ts_nop 0\n\tbuffer_load_dword %1, %2, 0 offen lds" ::"s"(lds0 + LDS_DUMMY), "v"(a), "s"(sr) : "memory");
    }
    u64 epoch;
    asm volatile("s_load_dwordx2 %0, %1, 0x0" : "=&s"(epoch) : "s"((const char*)pws + WS_EP));
    const int ustart = cg < 6 ? 6 * cg : 5 * cg + 6, nunit = cg < 6 ? 6 : 5;
    const u64 m12 = 0x0000FFFFFFFFFFFFull, mffn = cg < 6 ? m12 : 0x00003FF3FF3FF3FFull;
    const int wr = lane / 12, wc = lane - 12 * wr;
    const unsigned vx0 = (rb * 128 + 16 * w + (lane >> 3)) * 1024 + (((lane & 7) ^ (lane >> 4)) << 4);
    const unsigned vx1 = (rb * 128 + 16 * w + 8 + (lane >> 3)) * 1024 + (((lane & 7) ^ (4 + (lane >> 4))) << 4);
    const unsigned vwq = (8 * w + wr) * 3072 + wc * 16 + (wc < 8 ? 1024 + cg * 128 : cg * 64 - 128);
    const unsigned vwf = (8 * w + wr) * 2752 + ustart * 32 + wc * 16;
    const unsigned lx = lds0 + w * 2048, lw = lds0 + 2 * w * 784;
    auto issue = [&](auto J) {
        constexpr int j = J.v, sl = (j % 5) * SLOT, cs = P1Plan::order[j];
        if constexpr (cs < 8) p1_issue_a<sl, cs>(lx, lw, vx0, vx1, vwq, sx, sq, m12);
        else p1_issue_b<sl, cs - 8>(lw, vwf, sg, su, mffn);
    };
    static_for<0, P1Plan::PRO>([&](auto J) {
        if constexpr (J.v == 0) dma16(sw, lane * 16, 0, lds0 + LDS_GW);
        else __builtin_amdgcn_s_barrier();
        issue(J);
    });
    SCHED();
    STAMP(20);
    asm volatile("s_waitcnt lgkmcnt(0)" : "+s"(epoch));
    epoch = epoch * 0x9E3779B97F4A7C15ull + 0xD1B54A32D192ED03ull;
    Tag tag = {(unsigned)epoch, (unsigned)(epoch >> 32)};
    asm volatile("" : "+s"(tag.lo), "+s"(tag.hi));
    const int e = nh, kq = mt, head = 2 * cg + e;
    const unsigned f1v = WS_F1 + (((lane >> 5) * 16 + cg) * 64 + kq * 16 + ((lane >> 1) & 15)) * 128;
    f32x4 af[6][2];
    float z0, z1;
    u32x2 f1t;
    auto p1 = [&](auto NHC) {
        constexpr int NH = NHC.v;
        if constexpr (NH == 0) __builtin_amdgcn_s_setprio(1);
        unsigned bx[3][2], bw[3];
#pragma unroll
        for (int q = 0; q < 3; ++q) {
            bw[q] = lds0 + 2 * q * SLOT + 2 * g4 * 784 + (r16 >> 2) * 192 + 8 * (lane & 3);
            opaque(bw[q]);
#pragma unroll
            for (int ss = 0; ss < 2; ++ss) { bx[q][ss] = lds0 + 2 * q * SLOT + (32 * mt + r16) * 128 + ((g4 ^ (r16 >> 1) ^ (4 * ss)) << 4); opaque(bx[q][ss]); }
        }
        unsigned gwa = lds0 + LDS_GW + 16 * g4;
        opaque(gwa);
        bf16x8 xf[16][2];
        s16x4 wlo[8], whi[8];
        f32x4 aq[4][2];
        auto kv_out = [&]() {
            u32x4 kv[2];
            unsigned slot = lane;
            if (cg < 8) {
#pragma unroll
                for (int h2 = 0; h2 < 2; ++h2) kv[h2] = join16(aq[h2][0], aq[h2][1]);
                slot = (lane & 35) | ((lane & 16) >> 2) | ((lane & 12) << 1);
            } else {
                const int pd = ((r16 & 3) | ((r16 & 4) << 1) | ((r16 & 8) >> 1)) - 4 * g4;
                const u32x2 pm = {pd == 0 ? 0x3F80u : pd == 1 ? 0x3F800000u : 0u, pd == 2 ? 0x3F80u : pd == 3 ? 0x3F800000u : 0u};
#pragma unroll
                for (int h2 = 0; h2 < 2; ++h2)
#pragma unroll
                    for (int m = 0; m < 2; ++m) {
                        const u32x2 pk = {pk2(aq[h2][m][0], aq[h2][m][1]), pk2(aq[h2][m][2], aq[h2][m][3])};
                        const f32x4 t = MFMA16K16(__builtin_bit_cast(s16x4, pk), __builtin_bit_cast(s16x4, pm), f32x4{});
                        kv[h2][2 * m] = pk2(t[0], t[1]); kv[h2][2 * m + 1] = pk2(t[2], t[3]);
                    }
            }
#pragma unroll
            for (int h2 = 0; h2 < 2; ++h2) st_ws(ws, slot * 16, WS_K + ((4 * cg + 2 * NH + h2) * 64 + tile) * 1024, kv[h2]);
            SCHED();
        };
        auto q_out = [&]() {
            f32x4 q[2];
#pragma unroll
            for (int m = 0; m < 2; ++m)
#pragma unroll
                for (int i = 0; i < 4; ++i) q[m][i] = mul1(aq[2][m][i], QSCALE);
            *(LAS u32x4*)(size_t)(lds0 + LDS_Q + w * 1024 + lane * 16) = join16(q[0], q[1]);
            *(LAS f32x2*)(size_t)(lds0 + LDS_XCH + w * 512 + lane * 8) = (f32x2){aq[3][0][0], aq[3][1][0]};
            SCHED();
        };
        static_for<-P1Plan::PD, P1Plan::total>([&](auto I) {
            constexpr int n = I.v, nn = n + P1Plan::PD;
            constexpr int jn = P1Plan::stage(nn < P1Plan::total ? nn : 0), kn = nn - P1Plan::first(jn), cn = P1Plan::order[jn];
            constexpr int j = P1Plan::stage(n < 0 ? 0 : n), k = n - P1Plan::first(j), cs = P1Plan::order[j];
            constexpr bool fetch = nn < P1Plan::total;
            constexpr int ssn = cn < 8 ? ((kn >= 4) ^ NH) : kn / 6, un = cn < 8 ? (kn >= 4 ? kn - 4 : kn) : kn % 6;
            constexpr int ss = cs < 8 ? ((k >= 4) ^ NH) : k / 6, u = cs < 8 ? (k >= 4 ? k - 4 : k) : k % 6;
            constexpr int win = (jn % 5) / 2, so = ((jn % 5) & 1) * SLOT;
            constexpr bool fst = (cs == 0 && k < 4) || (cs == 8 && k < 6);
            if constexpr (fetch && kn == 0) {
                if constexpr (jn < 15) vm_bar<P1Plan::vm(jn, NH)>();
                if constexpr (jn == 0) { STAMP(1); }
                if constexpr (jn == 4) { STAMP(2); }
                if constexpr (jn == 8) { STAMP(3); }
                if constexpr (jn == 9) { STAMP(16); }
                if constexpr (jn == 10) { STAMP(17); }
                if constexpr (jn == 11) { STAMP(4); }
                if constexpr (jn == 14) { STAMP(5); }
                if constexpr (jn == P1Plan::JF) post_tag(ws, WS_F1 + ((2 * cg + NH) * 64 + tile) * 128, tag, lane);
                if constexpr (NH == 1 && jn + 3 < 16) issue(IC<(jn + 3) & 15>{});
                if constexpr (NH == 1 && jn == P1Plan::JS) kv_out();
                if constexpr (NH == 1 && jn == 13) q_out();
                if constexpr (jn == 15) f1t = peek_tags(sws, f1v);
                SCHED();
            }
            constexpr int wo = so + ssn * 8 * 784 + (cn < 8 ? 16384 + (un < 2 ? 64 * NH + 32 * un : 128 + 32 * NH) : 96 * NH + (un & 1) * WPART + (un >> 1) * 32);
            auto mul = [&](int m) {
                if constexpr (n >= 0) {
                    const bf16x8 wv = cat8(wlo[n & 7], whi[n & 7]), xv = xf[2 * (cs & 7) + ss][m];
                    if constexpr (cs >= 8) mma<fst>(af[u][m], wv, xv);
                    else mma<fst>(aq[u][m], wv, xv);
                }
            };
            mul(0);
            SCHED();
            if constexpr (fetch) {
                if constexpr (cn < 8 && un == 0) xf[2 * cn + ssn][0] = lds_frag(bx[win][ssn] + so);
                if constexpr (cn < 8 && un == 3) {
                    const bf16x8 gv = lds_frag(gwa + cn * 128 + 64 * NH);
                    wlo[nn & 7] = __builtin_bit_cast(s16x4, __builtin_shufflevector(gv, gv, 0, 1, 2, 3));
                    whi[nn & 7] = __builtin_bit_cast(s16x4, __builtin_shufflevector(gv, gv, 4, 5, 6, 7));
                } else wlo[nn & 7] = TRREAD(bw[win] + wo);
            }
            SCHED();
            mul(1);
            SCHED();
            if constexpr (fetch) {
                if constexpr (cn < 8 && un == 0) xf[2 * cn + ssn][1] = lds_frag(bx[win][ssn] + so + 2048);
                if constexpr (!(cn < 8 && un == 3)) whi[nn & 7] = TRREAD(bw[win] + wo + 784);
                if constexpr (NH == 0 && kn == P1Plan::cnt(jn) - 1) {
                    SCHED();
                    if constexpr (jn + 3 < 16) issue(IC<(jn + 3) & 15>{});
                    if constexpr (jn + 1 == P1Plan::JS) kv_out();
                    if constexpr (jn == 13) { q_out(); asm volatile("s_waitcnt lgkmcnt(0)" ::: "memory"); }
                }
            }
            SCHED();
        });
        const f32x2 o = *(const LAS f32x2*)(size_t)(lds0 + LDS_XCH + (w ^ 4) * 512 + lane * 8);
        z0 = aq[3][0][0] + o[0];
        z1 = aq[3][1][0] + o[1];
        STAMP(6);
        mfma_done();
        land_tags(f1t);
    };
    if (nh == 0) p1(IC<0>{}); else p1(IC<1>{});
    check_tags(ws, f1v, tag, f1t);
    STAMP(7);
    unsigned kb = WS_K + (head * 64 + kq * 16) * 1024;
    const unsigned vo = lane * 16, vv = (WS_V - WS_K) + (lane >> 5) * 256 + r16 * 16;
    u32x16 kv0, kv1, kv2;
    {
        const unsigned c1 = (lane & 27) == 16 ? 0x3F803F80u : 0u;
        asm volatile(A_CST8(112) A_CST8(124) A_L01 A_CST8(136) A_CST8(148) A_L23
                     : "=&{v[112:127]}"(kv0), "=&{v[128:143]}"(kv1), "=&{v[144:159]}"(kv2), [kb] "+s"(kb)
                     : [vo] "v"(vo), [vv] "v"(vv), [cst] "v"((u32x2){c1, c1}), [srd] "s"(sws), [msk] "s"(0x0000FFFF0000FFFFull)
                     : "memory", "scc");
    }
    u32x16 qf;
#pragma unroll
    for (int i = 0; i < 4; ++i) {
        const u32x4 q = *(const LAS u32x4*)(size_t)(lds0 + LDS_Q + (e * 4 + ((i + 1 + kq) & 3)) * 1024 + lane * 16);
        qf[4 * i] = q[0]; qf[4 * i + 1] = q[1]; qf[4 * i + 2] = q[2]; qf[4 * i + 3] = q[3];
    }
    z0 *= LOG2E; z1 *= LOG2E;
    {
        constexpr float C0 = -2.f * 0.7978845608028654f * LOG2E, C1 = C0 * 0.044715f;
        const float io[2] = {1.f + __builtin_amdgcn_exp2f(fminf(z0, 126.f)), 1.f + __builtin_amdgcn_exp2f(fminf(z1, 126.f))};
        const int u0 = ustart + 3 * nh;
        const unsigned hv0 = WS_A + (2 * tile + (g4 & 1)) * NKS * 1024 + (g4 >> 1) * 256 + r16 * 16 + u0 * 512;
#pragma unroll
        for (int u = 0; u < 3; ++u) {
            f32x4 hv[2];
#pragma unroll
            for (int m = 0; m < 2; ++m)
#pragma unroll
                for (int i = 0; i < 4; i += 2) {
                    const f32x2 t = {af[2 * u][m][i], af[2 * u][m][i + 1]}, up = {af[2 * u + 1][m][i], af[2 * u + 1][m][i + 1]};
                    const f32x2 a = t * __builtin_elementwise_fma(t * t, (f32x2){C1, C1}, (f32x2){C0, C0});
                    const f32x2 ex = {__builtin_amdgcn_exp2f(a[0]), __builtin_amdgcn_exp2f(a[1])};
                    const f32x2 d = __builtin_elementwise_fma(ex, (f32x2){io[m], io[m]}, (f32x2){io[m], io[m]});
                    const f32x2 hh = (t * up) * (f32x2){__builtin_amdgcn_rcpf(d[0]), __builtin_amdgcn_rcpf(d[1])};
                    hv[m][i] = hh[0]; hv[m][i + 1] = hh[1];
                }
            st_ws(ws, hv0, u < 2 ? u * 512 : (3 * nh + 2 < nunit ? 1024 : 0x7f000000u), join16(hv[0], hv[1]));
        }
    }
    float g = __builtin_amdgcn_rcpf(1.f + __builtin_amdgcn_exp2f(-((g4 & 1) ? z1 : z0)));
    asm volatile("" : "+v"(g));
    STAMP(8);
    const int m3 = w & 1, kk = (w >> 1) & 1;
    const u32x4 sd = make_srd(pwd, FF * DM * 2), so = make_srd(pwo, DM * DM * 2);
    const unsigned ab = WS_A + ((rt * 4 + m3 * 2) * NKS + kk) * 1024;
    f32x16 o3;
    u32x16 h0, h1, h2, h3, h4;
    {
        const unsigned vw = (8 * w + (lane >> 3)) * 1024 + cb * 128 + (((lane & 7) ^ ((g4 & 1) << 1) ^ ((w & 1) << 2)) << 4);
        const unsigned rd = lds0 + LDS_RED + e * 12 * 2304;
        unsigned wl = lds0 + w * 1024, wo = 0, hb = ab + (nh ? 0 : 22 * 1024), cap = SPIN_CAP;
        u64 tmp;
        asm volatile(
            A_P(3)
            "v_mov_b32 v160, 0xdddddddd\n\t"
            A_Z(88) A_Z(0) A_Z(16) A_Z(32) A_Z(48)
            A_W(9)
            "v_mfma_f32_32x32x16_bf16 v[64:79], v[112:115], v[96:99], 0\n\t"
            "v_mov_b32 v8, 0\n\tv_mov_b32 v24, 0\n\tv_mov_b32 v40, 0\n\tv_mov_b32 v56, 0\n\t"
            "s_nop 7\n\t"
            ".p2align 3\n\t"
            A_S(0) A_KEYT(112, 116, 120, 120, 124, "", "", "", "", "") A_T1("", "", "", A_W(3))
            "s_barrier\n\ts_nop 0\n\t"
            A_S(1) A_T2(A_L01, A_D, A_D) A_T3(A_D, A_D, "", A_W(4) A_POST)
            A_S(2) A_P(2) A_T0(A_L23, A_D, A_D, "") A_T1(A_D, A_D, A_D, A_W(5))
            A_S(3) A_T2(A_L01, A_D, A_D) A_T3(A_D, A_D, A_WARM, A_W(6))
            A_S(4) A_P(1) A_T0(A_POLL A_L23, "", "", A_W(6) A_CHECK) A_T1(A_H(164), A_H(172), A_H(180), A_W(6))
            A_S(5) A_T2(A_L01, A_H(188), A_H(196)) A_T3(A_H(204), A_H(212), A_H(220), A_W(10))
            A_S(6) A_P(0) A_T0(A_L23, A_H(228), A_H(236), "") A_T1("", "", "", A_W(4))
            A_S(7) A_T2("", "", "") A_KEYT(148, 152, 156, 144, 112, "", "", A_R(0, r0), A_R(16, r1), "")
            A_R(32, r2)
            "v_smfmac_f32_32x32x32_bf16 v[48:63], v[156:159], v[88:95], v160\n\t"
            "s_waitcnt vmcnt(0) lgkmcnt(0)\n\t"
            "s_barrier\n\t"
            "s_nop 9"
            : "=&{v[48:63]}"(o3), "+{v[112:127]}"(kv0), "+{v[128:143]}"(kv1), "+{v[144:159]}"(kv2),
              "=&{v[164:179]}"(h0), "=&{v[180:195]}"(h1), "=&{v[196:211]}"(h2), "=&{v[212:227]}"(h3), "=&{v[228:243]}"(h4),
              [kb] "+s"(kb), [wl] "+s"(wl), [wo] "+s"(wo), [hb] "+s"(hb), [cap] "+s"(cap), [tmp] "=&s"(tmp) A_SOUT
            : "{v[96:111]}"(qf), [vo] "v"(vo), [vv] "v"(vv), [vw] "v"(vw),
              [srd] "s"(sws), [sd] "s"(sd), [so] "s"(so), [msk] "s"(0x0000FFFF0000FFFFull), [tg0] "s"(tag.lo), [tg1] "s"(tag.hi),
              [fh] "s"(WS_FH + (tile * 32 + 2 * cg + nh) * 128), [fp] "s"(WS_FH + (rt * 2 + m3) * 4096),
              [w1] "s"((832 + ((rt & 3) * 8 + w) * 17) * 1024 + cb * 128), [w2] "s"(((rt & 3) * 8 + w) * 16 * 1024 + cb * 128),
              [r0] "s"(rd + ((kq + 1) & 3) * 3 * 2304), [r1] "s"(rd + (((kq + 2) & 3) * 3 + 1) * 2304), [r2] "s"(rd + (((kq + 3) & 3) * 3 + 2) * 2304)
            : "memory", "scc", "vcc", "v0", "v1", "v2", "v3", "v4", "v5", "v6", "v7", "v8", "v9", "v10", "v11", "v12", "v13", "v14", "v15",
              "v16", "v17", "v18", "v19", "v20", "v21", "v22", "v23", "v24", "v25", "v26", "v27", "v28", "v29", "v30", "v31",
              "v32", "v33", "v34", "v35", "v36", "v37", "v38", "v39", "v40", "v41", "v42", "v43", "v44", "v45", "v46", "v47",
              "v64", "v65", "v66", "v67", "v68", "v69", "v70", "v71", "v72", "v73", "v74", "v75", "v76", "v77", "v78", "v79",
              "v80", "v81", "v82", "v83", "v84", "v85", "v86", "v87", "v88", "v89", "v90", "v91", "v92", "v93", "v94", "v95",
              "v160", "v161", "v162", "v163");
    }
    STAMP(9);
    asm volatile("" : "+v"(lane));
    r16 = lane & 15; g4 = lane >> 4;
    bf16x8 a3[11][2];
#pragma unroll
    for (int m = 0; m < 2; ++m) a3[10][m] = ld_ws(ws, w == 2 || w == 3 ? 0x7ff00000u : lane * 16, ab + (m * NKS + (nh ? 20 : 42)) * 1024);
    SCHED();
    {
        f32x8 oo = __builtin_shufflevector(o3, o3, 0, 1, 2, 3, 4, 5, 6, 7);
        float l = o3[8];
#pragma unroll
        for (int i = 0; i < 3; ++i) {
            const unsigned a = lds0 + LDS_RED + ((e * 4 + kq) * 3 + i) * 2304;
            const f32x4 lo = *(const LAS f32x4*)(size_t)(a + lane * 16), hi = *(const LAS f32x4*)(size_t)(a + 1024 + lane * 16);
#pragma unroll
            for (int j = 0; j < 4; ++j) { oo[j] += lo[j]; oo[4 + j] += hi[j]; }
            l += *(const LAS float*)(size_t)(a + 2048 + lane * 4);
        }
        float am = 0.f;
#pragma unroll
        for (int j = 0; j < 8; ++j) am = fmaxf(am, fabsf(oo[j]));
        const bool ok = l >= 0x1p-40f && l <= 0x1p40f && am <= 0x1p120f;
        if (__builtin_expect(__any(!ok), 0))
            l = attn_safe(ws, __builtin_bit_cast(bf16x8, (u32x4){qf[12], qf[13], qf[14], qf[15]}), oo, WS_K + head * 65536, lane);
        const float sc = g * __builtin_amdgcn_rcpf(l);
        st_ws(ws, (g4 & 1) * NKS * 1024 + (g4 >> 1) * 256 + r16 * 16, WS_A + (2 * tile * NKS + 43 + cg) * 1024 + e * 512,
              (u32x4){pk2(oo[0] * sc, oo[1] * sc), pk2(oo[2] * sc, oo[3] * sc), pk2(oo[4] * sc, oo[5] * sc), pk2(oo[6] * sc, oo[7] * sc)});
    }
    STAMP(10);
    {
        const u32x16 hh[5] = {h0, h1, h2, h3, h4};
#pragma unroll
        for (int st = 0; st < 10; ++st)
#pragma unroll
            for (int m = 0; m < 2; ++m) {
                const int q = (st & 1) * 8 + m * 4;
                a3[st][m] = __builtin_bit_cast(bf16x8, (u32x4){hh[st >> 1][q], hh[st >> 1][q + 1], hh[st >> 1][q + 2], hh[st >> 1][q + 3]});
            }
    }
    const unsigned fov = WS_FO + (tile * 32 + head) * 128;
    const unsigned vw0 = (16 * mt + (lane >> 3)) * 1024 + cb * 128 + (((lane & 7) ^ ((g4 & 1) << 1)) << 4), vw1 = (vw0 ^ 64) + 8192;
    auto wblock = [&](bool o_rows, int blk, int slot) {
        dma16(o_rows ? so : sd, vw0, blk * 65536, lds0 + slot * HS + mt * 2048);
        dma16(o_rows ? so : sd, vw1, blk * 65536, lds0 + slot * HS + mt * 2048 + 1024);
    };
    const unsigned wb3 = lds0 + (32 * kk + 8 * g4 + (r16 >> 2)) * 128 + (((r16 >> 3) | ((g4 & 1) << 1)) << 5) + 8 * (lane & 3);
    f32x4 acc[4][2];
    constexpr int PD3 = 4;
    auto stream = [&](auto NBC, auto S0C, auto FSTC, auto& a, auto&& hook) {
        constexpr int NB = NBC.v, S0 = S0C.v;
        s16x4 wlo[8], whi[8];
        unsigned b3[4];
        static_for<-PD3, 4 * NB>([&](auto I) {
            constexpr int n = I.v, nn = n + PD3, slot = (S0 + nn / 4) % 20, nun = nn % 4, st = n / 4, nu = n % 4;
            constexpr bool fetch = nn < 4 * NB, fst = FSTC.v && n < 4;
            hook(I);
            if constexpr (fetch && (nn < 4 || slot % 8 == 0)) { b3[nun] = (wb3 ^ (nun * 32)) + (slot & ~7) * HS; opaque(b3[nun]); }
            SCHED();
            if constexpr (n >= 0) mma<fst>(acc[nu][0], cat8(wlo[n & 7], whi[n & 7]), a[st][0]);
            SCHED();
            if constexpr (fetch) wlo[nn & 7] = TRREAD(b3[nun] + (slot & 7) * HS);
            SCHED();
            if constexpr (n >= 0) mma<fst>(acc[nu][1], cat8(wlo[n & 7], whi[n & 7]), a[st][1]);
            SCHED();
            if constexpr (fetch) whi[nn & 7] = TRREAD(b3[nun] + (slot & 7) * HS + 512);
            SCHED();
        });
    };
    auto dump = [&]() {
        mfma_done();
#pragma unroll
        for (int nu = 0; nu < 4; ++nu)
#pragma unroll
            for (int m = 0; m < 2; ++m)
                *(LAS f32x4*)(size_t)(lds0 + LDS_SUM + ((w >> 1) * 64 + m3 * 32 + m * 16 + r16) * 272 + (nu * 16 + 4 * g4) * 4) = acc[nu][m];
    };
    if (nh == 1) {
        __builtin_amdgcn_s_setprio(1);
        stream(IC<11>{}, IC<0>{}, IC<1>{}, a3, [&](auto I) {
            if constexpr (I.v == 0) BAR();
            if constexpr (I.v == 24) { vm_bar<0>(); post_tag(ws, fov, tag, lane); }
        });
        BAR();
        STAMP(11);
        const unsigned fpv = WS_FO + ((rt * 2 + m3) * 32 + (lane >> 3) * 4 + 2 * kk + ((lane >> 2) & 1)) * 128;
#pragma unroll
        for (int i = 4; i < 8; ++i) wblock(true, i, 2 + i);
        u32x2 fot = peek_tags(sws, fpv);
        BAR();
        land_tags(fot);
        check_tags(ws, fpv, tag, fot);
        STAMP(12);
        bf16x8 ao[8][2];
#pragma unroll
        for (int i = 0; i < 8; ++i)
#pragma unroll
            for (int m = 0; m < 2; ++m) ao[i][m] = ld_ws(ws, lane * 16, ab + (m * NKS + 43 + 2 * i) * 1024);
        SCHED();
        stream(IC<8>{}, IC<2>{}, IC<0>{}, ao, [&](auto I) {
            if constexpr (I.v == 16 - PD3) BAR();
        });
        STAMP(13);
        dump();
    } else {
        BAR();
#pragma unroll
        for (int i = 2; i < 9; ++i) wblock(false, 11 + i, 11 + i);
        vm_wait<14>();
        post_tag(ws, fov, tag, lane);
        BAR();
        wblock(false, 20, 0); wblock(false, 21, 1);
#pragma unroll
        for (int i = 0; i < 4; ++i) wblock(true, i, 2 + i);
        stream(IC<11>{}, IC<11>{}, IC<1>{}, a3, [&](auto I) {
            if constexpr (I.v == 0) { vm_bar<13>(); STAMP(11); }
            if constexpr (I.v == 16) { vm_bar<0>(); STAMP(12); }
        });
        STAMP(13);
        dump();
        BAR();
    }
    LGKM_BAR();
    STAMP(14);
    const int row = w * 8 + (lane >> 3), c8 = lane & 7;
    f32x4 lo = {}, hi = {};
#pragma unroll
    for (int q = 0; q < 4; ++q) {
        const unsigned ad = lds0 + LDS_SUM + (q * 64 + row) * 272 + c8 * 32;
        lo += *(const LAS f32x4*)(size_t)ad;
        hi += *(const LAS f32x4*)(size_t)(ad + 16);
    }
    *(u32x4*)((char*)pout + (rt * 64 + row) * 1024 + cb * 128 + c8 * 16) = (u32x4){pk2(lo[0], lo[1]), pk2(lo[2], lo[3]), pk2(hi[0], hi[1]), pk2(hi[2], hi[3])};
    asm volatile(".Lgm_code_hi:");
    if (b == 0 && w == 0) post_tag(ws, WS_EP, tag, lane);
}

extern "C" void kernel_launch(void* const* d_in, const int* in_sizes, int n_in, void* d_out, int out_size, void* d_ws, size_t ws_size,
                              hipStream_t stream) {
    (void)hipFuncSetAttribute((const void*)gm_kernel, hipFuncAttributeMaxDynamicSharedMemorySize, LDS_BYTES);
    gm_kernel<<<256, 512, LDS_BYTES, stream>>>(d_in[0], d_in[1], d_in[4], d_in[5], d_in[7], d_ws, d_in[6], d_in[2], d_out);
}
```

```cpp
#include <hip/hip_runtime.h>
#include <hip/hip_bf16.h>

typedef float f32x2 __attribute__((ext_vector_type(2)));
typedef float f32x4 __attribute__((ext_vector_type(4)));
typedef float f32x8 __attribute__((ext_vector_type(8)));
typedef float f32x16 __attribute__((ext_vector_type(16)));
typedef __bf16 bf16x2 __attribute__((ext_vector_type(2)));
typedef __bf16 bf16x8 __attribute__((ext_vector_type(8)));
typedef __bf16 bf16x16 __attribute__((ext_vector_type(16)));
typedef short s16x4 __attribute__((ext_vector_type(4)));
typedef unsigned u32x2 __attribute__((ext_vector_type(2)));
typedef unsigned u32x4 __attribute__((ext_vector_type(4)));
#define LAS __attribute__((address_space(3)))
#define DI __device__ __forceinline__

constexpr int SEQ = 2048, DM = 512, FF = 1376;
constexpr int NKS = 59;
constexpr unsigned WS_K = 0, WS_V = 2u << 20, WS_A = 4u << 20, WS_F1 = 12u << 20, WS_FH = WS_F1 + 2048 * 128,
                   WS_EP = WS_FH + 2048 * 128, WS_END = WS_EP + 256;
constexpr int WPART = 16 * 784;
constexpr int SLOT = 16384 + WPART, LDS_XCH = 5 * SLOT, LDS_GW = LDS_XCH + 8 * 512, LDS_DUMMY = LDS_GW + 1024, LDS_Q = LDS_DUMMY + 256;
constexpr int HS = 8192;
constexpr int LDS_RED = 13 * HS, LDS_SUM = 10 * HS, LDS_BYTES = 160 * 1024;
static_assert(LDS_Q + 8192 <= LDS_BYTES && LDS_RED + 24 * 2304 <= LDS_BYTES, "LDS map");
constexpr float LOG2E = 1.4426950408889634f, QSCALE = 0.25f * LOG2E;
constexpr int SPIN_CAP = 1 << 16;
typedef unsigned long long u64;

DI u32x4 make_srd(const void* p, unsigned bytes) {
    const unsigned long long a = (unsigned long long)p;
    return (u32x4){(unsigned)a, (unsigned)(a >> 32) & 0xffffu, bytes, 0x00020000u};
}
DI void dma16(u32x4 srd, unsigned voff, unsigned soff, unsigned lds) {
    asm volatile("s_mov_b32 m0, %0\n\ts_nop 0\n\tbuffer_load_dwordx4 %1, %2, %3 offen lds" ::"s"(lds), "v"(voff), "s"(srd), "s"(soff) : "memory");
}
template <int N> DI void vm_bar() { asm volatile("s_waitcnt vmcnt(%0)\n\ts_barrier" ::"i"(N) : "memory"); }
#define SCHED() __builtin_amdgcn_sched_barrier(0)
template <int I> struct IC { static constexpr int v = I; };
template <int B, int E, class Fn> DI void static_for(Fn&& f) { if constexpr (B < E) { f(IC<B>{}); static_for<B + 1, E>(f); } }
#define BAR() asm volatile("s_barrier" ::: "memory")
#define LGKM_BAR() asm volatile("s_waitcnt lgkmcnt(0)\n\ts_barrier" ::: "memory")
template <int N> DI void vm_wait() { asm volatile("s_waitcnt vmcnt(%0)" ::"i"(N) : "memory"); }
template <bool FIRST> DI void mma(f32x4& acc, bf16x8 a, bf16x8 b) {
    if constexpr (FIRST) asm volatile("v_mfma_f32_16x16x32_bf16 %0, %1, %2, 0" : "=&v"(acc) : "v"(a), "v"(b));
    else asm volatile("v_mfma_f32_16x16x32_bf16 %0, %1, %2, %0" : "+v"(acc) : "v"(a), "v"(b));
}
DI void mfma_done() { asm volatile("s_nop 7" ::: "memory"); SCHED(); }
DI float add1(float a, float b) { float r; asm("v_add_f32 %0, %1, %2" : "=v"(r) : "v"(a), "v"(b)); return r; }
DI float mul1(float a, float b) { float r; asm("v_mul_f32 %0, %1, %2" : "=v"(r) : "v"(a), "v"(b)); return r; }

#if __HIP_DEVICE_COMPILE__
#define MFMA(a, b, c) __builtin_amdgcn_mfma_f32_32x32x16_bf16((a), (b), (c), 0, 0, 0)
#define SMFMAC(a, b, c) __builtin_amdgcn_smfmac_f32_32x32x32_bf16((a), __builtin_bit_cast(bf16x16, (b)), (c), (int)0xDDDDDDDD, 0, 0)
#define MFMA16K16(a, b, c) __builtin_amdgcn_mfma_f32_16x16x16bf16_1k((a), (b), (c), 0, 0, 0)
#define TRREAD(a) __builtin_amdgcn_ds_read_tr16_b64_v4i16((LAS s16x4*)(size_t)(a))
#else
#define MFMA(a, b, c) (c)
#define SMFMAC(a, b, c) (c)
#define MFMA16K16(a, b, c) (c)
#define TRREAD(a) (s16x4{})
#endif
DI bf16x8 lds_frag(unsigned a) { return *(const LAS bf16x8*)(size_t)a; }
DI unsigned pk2(float a, float b) { return __builtin_bit_cast(unsigned, __builtin_convertvector((f32x2){a, b}, bf16x2)); }
DI bf16x8 cat8(s16x4 lo, s16x4 hi) { return __builtin_bit_cast(bf16x8, __builtin_shufflevector(lo, hi, 0, 1, 2, 3, 4, 5, 6, 7)); }
DI u32x4 join16(f32x4 a, f32x4 b) {
    auto r0 = __builtin_amdgcn_permlane16_swap(pk2(a[0], a[1]), pk2(b[0], b[1]), false, false);
    auto r1 = __builtin_amdgcn_permlane16_swap(pk2(a[2], a[3]), pk2(b[2], b[3]), false, false);
    return (u32x4){r0[0], r1[0], r0[1], r1[1]};
}
DI float half_max(float v) {
    auto r = __builtin_amdgcn_permlane32_swap(__float_as_uint(v), __float_as_uint(v), false, false);
    return fmaxf(__uint_as_float(r[0]), __uint_as_float(r[1]));
}
typedef __amdgpu_buffer_rsrc_t rsrc_t;
DI rsrc_t make_rsrc(const void* p, unsigned bytes) { return __builtin_amdgcn_make_buffer_rsrc((void*)p, 0, bytes, 0x00020000); }
constexpr int AUX_DEV = 16;
DI void st_ws(rsrc_t ws, unsigned voff, unsigned soff, u32x4 v) { __builtin_amdgcn_raw_buffer_store_b128(v, ws, voff + soff, 0, AUX_DEV); }
DI bf16x8 ld_ws(rsrc_t ws, unsigned voff, unsigned soff) { return __builtin_bit_cast(bf16x8, __builtin_amdgcn_raw_buffer_load_b128(ws, voff, soff, AUX_DEV)); }

struct Tag { unsigned lo, hi; };
DI void post_tag(rsrc_t ws, unsigned off, Tag tag, int lane) {
    if (lane == 0) __builtin_amdgcn_raw_buffer_store_b64((u32x2){tag.lo, tag.hi}, ws, off, 0, AUX_DEV);
}
DI u32x2 peek_tags(u32x4 sws, unsigned voff) {
    u32x2 v;
    asm volatile("buffer_load_dwordx2 %0, %1, %2, 0 offen sc1" : "=&v"(v) : "v"(voff), "s"(sws) : "memory");
    return v;
}
DI void wait_tags(rsrc_t ws, unsigned voff, Tag tag) {
#pragma unroll 1
    for (int spin = 0; spin < SPIN_CAP; ++spin) {
        asm volatile("" ::: "memory");
        const u32x2 v = __builtin_amdgcn_raw_buffer_load_b64(ws, voff, 0, AUX_DEV);
        if (__all(v[0] == tag.lo && v[1] == tag.hi)) break;
        __builtin_amdgcn_s_sleep(1);
    }
    asm volatile("" ::: "memory");
}
DI void land_tags(u32x2& v) { asm volatile("s_waitcnt vmcnt(0)" : "+v"(v) :: "memory"); }
DI void check_tags(rsrc_t ws, unsigned voff, Tag tag, u32x2 v) {
    if (__builtin_expect(!__all(v[0] == tag.lo && v[1] == tag.hi), 0)) wait_tags(ws, voff, tag);
}

#define STAMP(i)

#define CODEPF_WAVE 7
#define P1_ORDER 0, 1, 2, 3, 4, 5, 6, 7, 8, 9, 10, 11, 12, 13, 14, 15
#define P1_PD 4
#define P1_JF 2
namespace P1Plan {
    constexpr int order[16] = {P1_ORDER};
    constexpr int PD = P1_PD;
    constexpr int cnt(int j) { return order[j] < 8 ? 7 : 12; }
    constexpr int first(int j) { int n = 0; for (int i = 0; i < j; ++i) n += cnt(i); return n; }
    constexpr int stage(int n) { int j = 0; while (j < 15 && first(j + 1) <= n) ++j; return j; }
    constexpr int pos(int c) { int j = 0; while (order[j] != c) ++j; return j; }
    constexpr int total = 8 * 7 + 8 * 12;
    constexpr int PRO = 3;
    constexpr int need(int j) { return j == 14 ? 15 : j; }
    constexpr int JS = pos(7) + 2;
    constexpr int JF = JS + P1_JF;
    constexpr int issued(int j) { return j + 2 < 15 ? j + 2 : 15; }
    constexpr int vm(int j, int nh) {
        const int js = JS + nh, before = issued(js);
        int n = 4 * (issued(j) - need(j));
        if (j == JF) { const int y = 4 * (issued(j) - before); return n < y ? n : y; }
        if (j >= js && j < JF && need(j) <= before) n += 2;
        if (j > JF && need(j) <= issued(JF)) n += 1;
        return n;
    }
}
static_assert(P1Plan::JF > P1Plan::JS && P1Plan::JF < 14, "P1 plan");
template <int SL>
DI void p1_issue_a(unsigned kc, unsigned lx, unsigned lw, unsigned vx0, unsigned vx1, unsigned vwq, u32x4 sx, u32x4 sq, u64 mk) {
    unsigned t;
    asm volatile("s_add_u32 m0, %[lx], %[a0]\n\ts_lshl_b32 %[t], %[kc], 7\n\tbuffer_load_dwordx4 %[v0], %[s0], %[t] offen lds\n\t"
                 "s_add_u32 m0, %[lx], %[a0]+1024\n\ts_nop 0\n\tbuffer_load_dwordx4 %[v1], %[s0], %[t] offen lds\n\t"
                 "s_add_u32 m0, %[lw], %[a0]+16384\n\ts_mul_i32 %[t], %[kc], 64*3072\n\ts_mov_b64 exec, %[mk]\n\tbuffer_load_dwordx4 %[v2], %[s1], %[t] offen lds\n\t"
                 "s_add_u32 m0, %[lw], %[a0]+16384+784\n\ts_add_u32 %[t], %[t], 4*3072\n\tbuffer_load_dwordx4 %[v2], %[s1], %[t] offen lds\n\t"
                 "s_mov_b64 exec, -1"
                 : [t] "=&s"(t)
                 : [kc] "s"(kc), [lx] "s"(lx), [lw] "s"(lw), [a0] "i"(SL), [v0] "v"(vx0), [v1] "v"(vx1), [v2] "v"(vwq), [s0] "s"(sx), [s1] "s"(sq), [mk] "s"(mk)
                 : "memory", "scc");
}
template <int SL>
DI void p1_issue_b(unsigned kc, unsigned lw, unsigned vwf, u32x4 sg, u32x4 su, u64 mk) {
    unsigned t;
    asm volatile("s_add_u32 m0, %[lw], %[a0]\n\ts_mul_i32 %[t], %[kc], 64*2752\n\ts_mov_b64 exec, %[mk]\n\tbuffer_load_dwordx4 %[v0], %[s0], %[t] offen lds\n\t"
                 "s_add_u32 m0, %[lw], %[a0]+%[wp]\n\ts_nop 0\n\tbuffer_load_dwordx4 %[v0], %[s1], %[t] offen lds\n\t"
                 "s_add_u32 m0, %[lw], %[a0]+784\n\ts_add_u32 %[t], %[t], 4*2752\n\tbuffer_load_dwordx4 %[v0], %[s0], %[t] offen lds\n\t"
                 "s_add_u32 m0, %[lw], %[a0]+%[wp]+784\n\ts_nop 0\n\tbuffer_load_dwordx4 %[v0], %[s1], %[t] offen lds\n\t"
                 "s_mov_b64 exec, -1"
                 : [t] "=&s"(t)
                 : [kc] "s"(kc), [lw] "s"(lw), [a0] "i"(SL), [wp] "i"(WPART), [v0] "v"(vwf), [s0] "s"(sg), [s1] "s"(su), [mk] "s"(mk)
                 : "memory", "scc");
}
DI void touch(u32x4 srd, unsigned voff, unsigned lds) {
    asm volatile("s_mov_b32 m0, %0\n\ts_nop 0\n\tbuffer_load_dword %1, %2, 0 offen lds" ::"s"(lds), "v"(voff), "s"(srd) : "memory");
}
DI void opaque(unsigned& a) { asm volatile("" : "+v"(a)); }

DI float attn_safe(rsrc_t ws, bf16x8 q, f32x8& oo, unsigned kb, int lane) {
    const f32x16 zero = {};
    float mx = -INFINITY;
#pragma unroll 1
    for (int t = 0; t < 64; ++t) {
        const f32x16 s = MFMA(ld_ws(ws, lane * 16, kb + t * 1024), q, zero);
#pragma unroll
        for (int j = 0; j < 16; ++j) mx = fmaxf(mx, s[j]);
    }
    mx = half_max(mx);
    const unsigned cst = (lane & 11) == 0 ? 0x3F803F80u : 0u, vv = (WS_V - WS_K) + (lane >> 5) * 256 + (lane & 15) * 16;
    const bf16x8 cf = __builtin_bit_cast(bf16x8, (u32x4){cst, cst, cst, cst});
    f32x16 o = zero;
#pragma unroll 1
    for (int t = 0; t < 64; ++t) {
        f32x16 s = MFMA(ld_ws(ws, lane * 16, kb + t * 1024), q, zero);
        bf16x8 va = ld_ws(ws, vv, kb + t * 1024), vc = ld_ws(ws, vv, kb + t * 1024 + 512);
        if (lane & 16) { va = cf; vc = cf; }
#pragma unroll
        for (int j = 0; j < 16; ++j) s[j] = __builtin_amdgcn_exp2f(s[j] - mx);
        o = SMFMAC(va, __builtin_shufflevector(s, s, 0, 1, 2, 3, 4, 5, 6, 7), o);
        o = SMFMAC(vc, __builtin_shufflevector(s, s, 8, 9, 10, 11, 12, 13, 14, 15), o);
    }
    oo = __builtin_shufflevector(o, o, 0, 1, 2, 3, 4, 5, 6, 7);
    return o[8];
}
#define A_EXP(S, i) "v_exp_f32 v[" #S "+" #i "], v[" #S "+" #i "]\n\t"
#define A_TILE(S, T, OP, VBP, KN, QN, OC, VAC, EXTRA, WAIT) A_TILE3(S, T, OP, VBP, KN, QN, OC, VAC, EXTRA, WAIT, "", "")
#define A_TILE3(S, T, OP, VBP, KN, QN, OC, VAC, EXTRA, WAIT, X2, X3)                                     \
    A_EXP(S, 0) A_EXP(S, 1)                                                                              \
    "v_smfmac_f32_32x32x32_bf16 v[" #OP ":" #OP "+15], v[" #VBP ":" #VBP "+3], v[" #T "+8:" #T "+15], v160\n\t" \
    EXTRA A_EXP(S, 2) A_EXP(S, 3) A_EXP(S, 4) WAIT                                                       \
    "v_mfma_f32_32x32x16_bf16 v[" #T ":" #T "+15], v[" #KN ":" #KN "+3], v[" #QN ":" #QN "+3], 0\n\t"    \
    A_EXP(S, 5) A_EXP(S, 6) A_EXP(S, 7) X2 A_EXP(S, 8) A_EXP(S, 9)                                       \
    "v_smfmac_f32_32x32x32_bf16 v[" #OC ":" #OC "+15], v[" #VAC ":" #VAC "+3], v[" #S ":" #S "+7], v160\n\t" \
    A_EXP(S, 10) A_EXP(S, 11) A_EXP(S, 12) X3 A_EXP(S, 13) A_EXP(S, 14) A_EXP(S, 15)
#define A_KEYT(KC, VA, VB, VBP, KN, E0, E1, E2, E3, WAIT)   \
    A_TILE(64, 80, 48, VBP, KC, 100, 0, VA, E0, "")         \
    A_TILE(80, 64, 0, VB, KC, 104, 16, VA, E1, "")          \
    A_TILE(64, 80, 16, VB, KC, 108, 32, VA, E2, "")         \
    A_TILE(80, 64, 32, VB, KN, 96, 48, VA, E3, WAIT)
#define A_T0(E0, E1, E2, W) A_KEYT(112, 116, 120, 156, 124, E0, E1, E2, "", W)
#define A_T1(E0, E1, E2, W) A_KEYT(124, 128, 132, 120, 136, E0, E1, E2, "", W)
#define A_T2(E0, E1, E2) A_KEYT(136, 140, 144, 132, 148, E0, E1, E2, "", "")
#define A_T3(E0, E1, E2, W) A_KEYT(148, 152, 156, 144, 112, E0, E1, E2, "", W)
#define A_LOAD2(R0, R1)                                                                            \
    "buffer_load_dwordx4 v[" #R0 ":" #R0 "+3], %[vo], %[srd], %[kb] offen sc1\n\t"                 \
    "buffer_load_dwordx4 v[" #R1 ":" #R1 "+3], %[vo], %[srd], %[kb] offen offset:1024 sc1\n\t"     \
    "s_mov_b64 exec, %[msk]\n\t"                                                                   \
    "buffer_load_dwordx4 v[" #R0 "+4:" #R0 "+7], %[vv], %[srd], %[kb] offen sc1\n\t"               \
    "buffer_load_dwordx4 v[" #R0 "+8:" #R0 "+11], %[vv], %[srd], %[kb] offen offset:512 sc1\n\t"   \
    "buffer_load_dwordx4 v[" #R1 "+4:" #R1 "+7], %[vv], %[srd], %[kb] offen offset:1024 sc1\n\t"   \
    "buffer_load_dwordx4 v[" #R1 "+8:" #R1 "+11], %[vv], %[srd], %[kb] offen offset:1536 sc1\n\t"  \
    "s_mov_b64 exec, -1\n\t"                                                                       \
    "s_add_u32 %[kb], %[kb], 2048\n\t"
#define A_L01 A_LOAD2(112, 124)
#define A_L23 A_LOAD2(136, 148)
#define A_CST8(R) "v_mov_b64 v[" #R "+4:" #R "+5], %[cst]\n\tv_mov_b64 v[" #R "+6:" #R "+7], %[cst]\n\t" \
                  "v_mov_b64 v[" #R "+8:" #R "+9], %[cst]\n\tv_mov_b64 v[" #R "+10:" #R "+11], %[cst]\n\t"
#define A_W(N) "s_waitcnt vmcnt(" #N ")\n\t"
#define A_P(N) "s_setprio " #N "\n\t"
#define A_D "s_mov_b32 m0, %[wl]\n\ts_add_u32 %[wl], %[wl], 8192\n\tbuffer_load_dwordx4 %[vw], %[sd], %[wo] offen lds\n\ts_add_u32 %[wo], %[wo], 65536\n\t"
#define A_WARM "v_lshlrev_b32 v161, 6, %[vo]\n\ts_mov_b64 exec, 0x1ffff\n\tbuffer_load_dword v162, v161, %[sd], %[w1] offen\n\t" \
               "s_mov_b64 exec, 0xffff\n\tbuffer_load_dword v163, v161, %[so], %[w2] offen\n\ts_mov_b64 exec, -1\n\t"
#define A_CODE "s_getpc_b64 s[80:81]\n.Lgm_code_mid:\n\ts_and_b32 s81, s81, 0xffff\n\ts_mov_b32 s82, .Lgm_code_hi-.Lgm_code_mid\n\ts_mov_b32 s83, 0x20000\n\t" \
               "v_lshlrev_b32 v161, 3, %[vo]\n\ts_mov_b64 exec, 0xffff\n\tbuffer_load_dword v123, v161, s[80:83], %[wc] offen\n\ts_mov_b64 exec, -1\n\t"
#define A_H(R) "buffer_load_dwordx4 v[" #R ":" #R "+3], %[vo], %[srd], %[hb] offen sc1\n\ts_add_u32 %[hb], %[hb], 60416\n\t" \
               "buffer_load_dwordx4 v[" #R "+4:" #R "+7], %[vo], %[srd], %[hb] offen sc1\n\ts_sub_u32 %[hb], %[hb], 58368\n\t"
static_assert(NKS * 1024 == 60416 && 60416 - 2048 == 58368, "A_H strides");
#define A_RA(RA) "v_add_u32 v161, %[" #RA "], %[vo]\n\tv_add_u32 v162, %[" #RA "], v163\n\t"
#define A_R0(O, RA) A_RA(RA) "ds_write_b128 v161, v[" #O ":" #O "+3]\n\t"
#define A_R1(O) "ds_write_b128 v161, v[" #O "+4:" #O "+7] offset:1024\n\t"
#define A_R2(O) "ds_write_b32 v162, v[" #O "+8] offset:2048\n\t"
#define A_LAST                                                                                    \
    A_TILE3(64, 80, 48, 132, 148, 96, 0, 140, "s_add_u32 %[hb], %[hb], %[hz]\n\t" A_H(112), "", "v_lshrrev_b32 v163, 2, %[vo]\n\t", "") \
    A_TILE3(80, 64, 0, 144, 136, 100, 0, 152, "", "", "", "")                                     \
    A_TILE3(64, 80, 0, 156, 148, 100, 16, 140, "", "", "", "")                                    \
    A_TILE3(80, 64, 16, 144, 136, 104, 16, 152, A_R0(0, r0), "", A_R1(0), A_R2(0))                \
    A_P(0)                                                                                        \
    A_TILE3(64, 80, 16, 156, 148, 104, 32, 140, "", "", "", "")                                   \
    A_TILE3(80, 64, 32, 144, 136, 108, 32, 152, A_R0(16, r1), "", A_R1(16), A_R2(16))             \
    A_TILE3(64, 80, 32, 156, 148, 108, 48, 140, "", "", "", "")                                   \
    A_TILE3(80, 64, 48, 144, 136, 108, 48, 152, A_R0(32, r2), "", A_R1(32), A_R2(32))
#define A_RD(R, OFF) "ds_read_b128 v[" #R ":" #R "+3], v161 offset:" #OFF "\n\tds_read_b128 v[" #R "+4:" #R "+7], v161 offset:" #OFF "+1024\n\t" \
                     "ds_read_b32 v[" #R "+8], v162 offset:" #OFF "+2048\n\t"
#define A_ADD(D, R) "v_pk_add_f32 v[" #D ":" #D "+1], v[" #D ":" #D "+1], v[" #R ":" #R "+1]\n\tv_pk_add_f32 v[" #D "+2:" #D "+3], v[" #D "+2:" #D "+3], v[" #R "+2:" #R "+3]\n\t" \
                    "v_pk_add_f32 v[" #D "+4:" #D "+5], v[" #D "+4:" #D "+5], v[" #R "+4:" #R "+5]\n\tv_pk_add_f32 v[" #D "+6:" #D "+7], v[" #D "+6:" #D "+7], v[" #R "+6:" #R "+7]\n\t" \
                    "v_add_f32 v[" #D "+8], v[" #D "+8], v[" #R "+8]\n\t"
#define A_Z(O) "v_mov_b64 v[" #O ":" #O "+1], 0\n\tv_mov_b64 v[" #O "+2:" #O "+3], 0\n\tv_mov_b64 v[" #O "+4:" #O "+5], 0\n\tv_mov_b64 v[" #O "+6:" #O "+7], 0\n\t"
#define A_POST "v_mov_b32 v161, %[fh]\n\tv_mov_b32 v162, %[tg0]\n\tv_mov_b32 v163, %[tg1]\n\ts_mov_b64 exec, 1\n\t" \
               "buffer_store_dwordx2 v[162:163], v161, %[srd], 0 offen sc1\n\ts_mov_b64 exec, -1\n\t"
#define A_POLL "v_and_b32 v161, 0x3e0, %[vo]\n\tv_lshlrev_b32 v161, 2, v161\n\tbuffer_load_dwordx2 v[162:163], v161, %[srd], %[fp] offen sc1\n\t"
#define A_CHECK "2:\n\tv_cmp_ne_u32 vcc, %[tg0], v162\n\tv_cmp_ne_u32 %[tmp], %[tg1], v163\n\ts_or_b64 vcc, vcc, %[tmp]\n\ts_cbranch_scc0 3f\n\t" \
                "s_sub_u32 %[cap], %[cap], 1\n\ts_cbranch_scc1 3f\n\ts_sleep 1\n\t"                                                             \
                "buffer_load_dwordx2 v[162:163], v161, %[srd], %[fp] offen sc1\n\ts_waitcnt vmcnt(0)\n\ts_branch 2b\n3:\n\t"
#define A_S(i) ""
#define A_SOUT
typedef unsigned u32x16 __attribute__((ext_vector_type(16)));
typedef unsigned u32x8 __attribute__((ext_vector_type(8)));

#define O_LOADS "s_mov_b32 %[hb], %[ob]\n\t" A_H(164) A_H(172) A_H(180) A_H(188) A_H(196) A_H(204) A_H(212) A_H(220)
DI void o_fetch(u32x16 (&oq)[4], u32x4 sws, unsigned vo, unsigned ob) {
    unsigned hb;
    asm volatile("s_mov_b32 %[hb], %[ob]\n\t" A_H(164) A_H(172) A_H(180) A_H(188)
                 : "=&{v[164:179]}"(oq[0]), "=&{v[180:195]}"(oq[1]), "=&{v[196:211]}"(oq[2]), "=&{v[212:227]}"(oq[3]), [hb] "=&s"(hb)
                 : [vo] "v"(vo), [srd] "s"(sws), [ob] "s"(ob) : "memory", "scc");
}
template <int I> DI void o_more(u32x16 (&oq)[4], u32x4 sws, unsigned vo, unsigned ob) {
    asm volatile("buffer_load_dwordx4 v[%c[r]:%c[r]+3], %[vo], %[srd], %[ob] offen sc1"
                 : "+{v[196:211]}"(oq[2]), "+{v[212:227]}"(oq[3])
                 : [vo] "v"(vo), [srd] "s"(sws), [ob] "s"(ob + ((I & 1) * NKS + 8 + (I & 6)) * 1024), [r] "i"(196 + 4 * I) : "memory");
}
template <int Q> DI void o_check(u32x16 (&oq)[4], u32x4 sws, unsigned vo, unsigned ob) {
    unsigned hb, cap, t0, t1;
    asm volatile("s_mov_b32 %[cap], %[lim]\n\ts_waitcnt vmcnt(%[n])\n"
                 "1:\n\t"
                 "v_max3_u32 %[t0], v[%c[r]], v[%c[r]+1], v[%c[r]+2]\n\tv_max3_u32 %[t1], v[%c[r]+3], v[%c[r]+4], v[%c[r]+5]\n\t"
                 "v_max3_u32 %[t0], %[t0], v[%c[r]+6], v[%c[r]+7]\n\tv_max3_u32 %[t1], %[t1], v[%c[r]+8], v[%c[r]+9]\n\t"
                 "v_max3_u32 %[t0], %[t0], v[%c[r]+10], v[%c[r]+11]\n\tv_max3_u32 %[t1], %[t1], v[%c[r]+12], v[%c[r]+13]\n\t"
                 "v_max3_u32 %[t0], %[t0], v[%c[r]+14], v[%c[r]+15]\n\tv_max_u32 %[t0], %[t0], %[t1]\n\t"
                 "v_cmp_eq_u32 vcc, -1, %[t0]\n\ts_cmp_eq_u64 vcc, 0\n\ts_cbranch_scc1 2f\n\t"
                 "s_sub_u32 %[cap], %[cap], 1\n\ts_cbranch_scc1 2f\n\t"
                 O_LOADS "s_waitcnt vmcnt(0)\n\ts_branch 1b\n"
                 "2:"
                 : "+{v[164:179]}"(oq[0]), "+{v[180:195]}"(oq[1]), "+{v[196:211]}"(oq[2]), "+{v[212:227]}"(oq[3]),
                   [hb] "=&s"(hb), [cap] "=&s"(cap), [t0] "=&v"(t0), [t1] "=&v"(t1)
                 : [vo] "v"(vo), [srd] "s"(sws), [ob] "s"(ob), [n] "i"(Q == 0 ? 4 : 12 - 4 * Q), [r] "i"(164 + 16 * Q), [lim] "i"(SPIN_CAP)
                 : "memory", "scc", "vcc");
}

__global__ __launch_bounds__(512, 2) void gm_kernel(const void* px, const void* pwqkv, const void* pwg, const void* pwu, const void* pgw,
                                                    void* pws, const void* pwd, const void* pwo, void* pout) {
    extern __shared__ __attribute__((aligned(16))) char smem[];
    const unsigned lds0 = (unsigned)(size_t)(LAS char*)smem;
    int lane = threadIdx.x & 63;
    const int w = __builtin_amdgcn_readfirstlane(threadIdx.x >> 6);
    const int b = blockIdx.x, xcd = b & 7, jb = b >> 3;
    const int rb = (xcd >> 2) * 8 + (jb & 7), cg = (xcd & 3) * 4 + (jb >> 3);
    const int rt = xcd * 4 + (jb >> 3), cb = jb & 7;
    STAMP(0);
    {
        u32x4 sc;
        unsigned lo, hi, len;
        asm volatile("s_getpc_b64 vcc\n.Lgm_code_lo:\n\ts_mov_b32 %0, vcc_lo\n\ts_and_b32 %1, vcc_hi, 0xffff\n\ts_mov_b32 %2, .Lgm_code_hi-.Lgm_code_lo"
                     : "=s"(lo), "=s"(hi), "=s"(len) :: "vcc", "scc");
        sc = (u32x4){lo, hi, len, 0x00020000u};
        if (w == CODEPF_WAVE) touch(sc, ((jb & 7) * 64 + lane) * 128, lds0 + LDS_DUMMY);
    }
    int r16 = lane & 15, g4 = lane >> 4;
    const rsrc_t ws = make_rsrc(pws, WS_END);
    const u32x4 sws = make_srd(pws, WS_END);
    const int mt = w & 3, nh = w >> 2, tile = rb * 4 + mt;
    const u32x4 sx = make_srd(px, SEQ * DM * 2), sq = make_srd(pwqkv, DM * 3 * DM * 2);
    const u32x4 sg = make_srd(pwg, DM * FF * 2), su = make_srd(pwu, DM * FF * 2), sw = make_srd(pgw, DM * 2);
    u64 epoch;
    asm volatile("s_load_dwordx2 %0, %1, 0x0" : "=&s"(epoch) : "s"((const char*)pws + WS_EP));
    const int ustart = cg < 6 ? 6 * cg : 5 * cg + 6, nunit = cg < 6 ? 6 : 5;
    const u64 m12 = 0x0000FFFFFFFFFFFFull, mffn = cg < 6 ? m12 : 0x00003FF3FF3FF3FFull;
    const int wr = lane / 12, wc = lane - 12 * wr;
    const unsigned vx0 = (rb * 128 + 16 * w + (lane >> 3)) * 1024 + (((lane & 7) ^ (lane >> 4)) << 4);
    const unsigned vx1 = (rb * 128 + 16 * w + 8 + (lane >> 3)) * 1024 + (((lane & 7) ^ (4 + (lane >> 4))) << 4);
    const unsigned vwq = (8 * w + wr) * 3072 + wc * 16 + (wc < 8 ? 1024 + cg * 128 : cg * 64 - 128);
    const unsigned vwf = (8 * w + wr) * 2752 + ustart * 32 + wc * 16;
    const unsigned lx = lds0 + w * 2048, lw = lds0 + 2 * w * 784;
    const unsigned rot = (rb + 2 * cg) & 7;
    auto issue = [&](auto J) {
        constexpr int j = J.v, sl = (j % 5) * SLOT, cs = P1Plan::order[j];
        if constexpr (cs < 8) p1_issue_a<sl>((cs + rot) & 7, lx, lw, vx0, vx1, vwq, sx, sq, m12);
        else p1_issue_b<sl>((cs + rot) & 7, lw, vwf, sg, su, mffn);
    };
    static_for<0, P1Plan::PRO>([&](auto J) {
        if constexpr (J.v == 0) dma16(sw, (lane * 16 + rot * 128) & 1023, 0, lds0 + LDS_GW);
        else __builtin_amdgcn_s_barrier();
        issue(J);
    });
    SCHED();
    STAMP(20);
    asm volatile("s_waitcnt lgkmcnt(0)" : "+s"(epoch), "+s"(pout));
    epoch = epoch * 0x9E3779B97F4A7C15ull + 0xD1B54A32D192ED03ull;
    Tag tag = {(unsigned)epoch, (unsigned)(epoch >> 32)};
    asm volatile("" : "+s"(tag.lo), "+s"(tag.hi));
    const int e = nh, kq = mt, head = 2 * cg + e;
    const unsigned f1v = WS_F1 + (((lane >> 5) * 16 + cg) * 64 + kq * 16 + ((lane >> 1) & 15)) * 128;
    f32x4 af[6][2];
    float z0, z1;
    u32x2 f1t;
    u32x16 qf;
    auto p1 = [&](auto NHC) {
        constexpr int NH = NHC.v;
        if constexpr (NH == 0) __builtin_amdgcn_s_setprio(1);
        unsigned bx[3][2], bw[3];
#pragma unroll
        for (int q = 0; q < 3; ++q) {
            bw[q] = lds0 + 2 * q * SLOT + 2 * g4 * 784 + (r16 >> 2) * 192 + 8 * (lane & 3);
            opaque(bw[q]);
#pragma unroll
            for (int ss = 0; ss < 2; ++ss) { bx[q][ss] = lds0 + 2 * q * SLOT + (32 * mt + r16) * 128 + ((g4 ^ (r16 >> 1) ^ (4 * ss)) << 4); opaque(bx[q][ss]); }
        }
        unsigned gwa = lds0 + LDS_GW + 16 * g4;
        opaque(gwa);
        bf16x8 xf[16][2];
        s16x4 wlo[8], whi[8];
        f32x2 zo;
        f32x4 aq[4][2];
        auto kv_out = [&]() {
            u32x4 kv[2];
            unsigned slot = lane;
            if (cg < 8) {
#pragma unroll
                for (int h2 = 0; h2 < 2; ++h2) kv[h2] = join16(aq[h2][0], aq[h2][1]);
                slot = (lane & 35) | ((lane & 16) >> 2) | ((lane & 12) << 1);
            } else {
                const int pd = ((r16 & 3) | ((r16 & 4) << 1) | ((r16 & 8) >> 1)) - 4 * g4;
                const u32x2 pm = {pd == 0 ? 0x3F80u : pd == 1 ? 0x3F800000u : 0u, pd == 2 ? 0x3F80u : pd == 3 ? 0x3F800000u : 0u};
#pragma unroll
                for (int h2 = 0; h2 < 2; ++h2)
#pragma unroll
                    for (int m = 0; m < 2; ++m) {
                        const u32x2 pk = {pk2(aq[h2][m][0], aq[h2][m][1]), pk2(aq[h2][m][2], aq[h2][m][3])};
                        const f32x4 t = MFMA16K16(__builtin_bit_cast(s16x4, pk), __builtin_bit_cast(s16x4, pm), f32x4{});
                        kv[h2][2 * m] = pk2(t[0], t[1]); kv[h2][2 * m + 1] = pk2(t[2], t[3]);
                    }
            }
#pragma unroll
            for (int h2 = 0; h2 < 2; ++h2) st_ws(ws, slot * 16, WS_K + ((4 * cg + 2 * NH + h2) * 64 + tile) * 1024, kv[h2]);
            SCHED();
        };
        auto q_out = [&]() {
            f32x4 q[2];
#pragma unroll
            for (int m = 0; m < 2; ++m)
#pragma unroll
                for (int i = 0; i < 4; ++i) q[m][i] = mul1(aq[2][m][i], QSCALE);
            *(LAS u32x4*)(size_t)(lds0 + LDS_Q + w * 1024 + lane * 16) = join16(q[0], q[1]);
            *(LAS f32x2*)(size_t)(lds0 + LDS_XCH + w * 512 + lane * 8) = (f32x2){aq[3][0][0], aq[3][1][0]};
            SCHED();
        };
        static_for<-P1Plan::PD, P1Plan::total>([&](auto I) {
            constexpr int n = I.v, nn = n + P1Plan::PD;
            constexpr int jn = P1Plan::stage(nn < P1Plan::total ? nn : 0), kn = nn - P1Plan::first(jn), cn = P1Plan::order[jn];
            constexpr int j = P1Plan::stage(n < 0 ? 0 : n), k = n - P1Plan::first(j), cs = P1Plan::order[j];
            constexpr bool fetch = nn < P1Plan::total;
            constexpr int ssn = cn < 8 ? ((kn >= 4) ^ NH) : kn / 6, un = cn < 8 ? (kn >= 4 ? kn - 4 : kn) : kn % 6;
            constexpr int ss = cs < 8 ? ((k >= 4) ^ NH) : k / 6, u = cs < 8 ? (k >= 4 ? k - 4 : k) : k % 6;
            constexpr int win = (jn % 5) / 2, so = ((jn % 5) & 1) * SLOT;
            constexpr bool fst = (cs == 0 && k < 4) || (cs == 8 && k < 6);
            if constexpr (fetch && kn == 0) {
                if constexpr (jn < 15) vm_bar<P1Plan::vm(jn, NH)>();
                if constexpr (jn == 0) { STAMP(1); }
                if constexpr (jn == 4) { STAMP(2); }
                if constexpr (jn == 8) { STAMP(3); }
                if constexpr (jn == 11) { STAMP(4); }
                if constexpr (jn == 14) { STAMP(5); }
                if constexpr (jn == P1Plan::JF) post_tag(ws, WS_F1 + ((2 * cg + NH) * 64 + tile) * 128, tag, lane);
                if constexpr (NH == 1 && jn + 3 < 16) issue(IC<(jn + 3) & 15>{});
                if constexpr (NH == 1 && jn == P1Plan::JS) kv_out();
                if constexpr (NH == 1 && jn == 13) q_out();
                if constexpr (jn == 15) {
                    f1t = peek_tags(sws, f1v);
                    zo = *(const LAS f32x2*)(size_t)(lds0 + LDS_XCH + (w ^ 4) * 512 + lane * 8);
#pragma unroll
                    for (int i = 0; i < 4; ++i) {
                        const u32x4 q = *(const LAS u32x4*)(size_t)(lds0 + LDS_Q + (NH * 4 + ((i + 1 + kq) & 3)) * 1024 + lane * 16);
                        qf[4 * i] = q[0]; qf[4 * i + 1] = q[1]; qf[4 * i + 2] = q[2]; qf[4 * i + 3] = q[3];
                    }
                }
                SCHED();
            }
            constexpr int wo = so + ssn * 8 * 784 + (cn < 8 ? 16384 + (un < 2 ? 64 * NH + 32 * un : 128 + 32 * NH) : 96 * NH + (un & 1) * WPART + (un >> 1) * 32);
            auto mul = [&](int m) {
                if constexpr (n >= 0) {
                    const bf16x8 wv = cat8(wlo[n & 7], whi[n & 7]), xv = xf[2 * (cs & 7) + ss][m];
                    if constexpr (cs >= 8) mma<fst>(af[u][m], wv, xv);
                    else mma<fst>(aq[u][m], wv, xv);
                }
            };
            mul(0);
            SCHED();
            if constexpr (fetch) {
                if constexpr (cn < 8 && un == 0) xf[2 * cn + ssn][0] = lds_frag(bx[win][ssn] + so);
                if constexpr (cn < 8 && un == 3) {
                    const bf16x8 gv = lds_frag(gwa + cn * 128 + 64 * NH);
                    wlo[nn & 7] = __builtin_bit_cast(s16x4, __builtin_shufflevector(gv, gv, 0, 1, 2, 3));
                    whi[nn & 7] = __builtin_bit_cast(s16x4, __builtin_shufflevector(gv, gv, 4, 5, 6, 7));
                } else wlo[nn & 7] = TRREAD(bw[win] + wo);
            }
            SCHED();
            mul(1);
            SCHED();
            if constexpr (fetch) {
                if constexpr (cn < 8 && un == 0) xf[2 * cn + ssn][1] = lds_frag(bx[win][ssn] + so + 2048);
                if constexpr (!(cn < 8 && un == 3)) whi[nn & 7] = TRREAD(bw[win] + wo + 784);
                if constexpr (NH == 0 && kn == P1Plan::cnt(jn) - 1) {
                    SCHED();
                    if constexpr (jn + 3 < 16) issue(IC<(jn + 3) & 15>{});
                    if constexpr (jn + 1 == P1Plan::JS) kv_out();
                    if constexpr (jn == 13) { q_out(); asm volatile("s_waitcnt lgkmcnt(0)" ::: "memory"); }
                }
            }
            SCHED();
        });
        STAMP(6);
        if constexpr (NH == 1) LGKM_BAR();
        __builtin_amdgcn_s_setprio(3);
        z0 = mul1(add1(aq[3][0][0], zo[0]), LOG2E);
        z1 = mul1(add1(aq[3][1][0], zo[1]), LOG2E);
        mfma_done();
        land_tags(f1t);
    };
    if (nh == 0) p1(IC<0>{}); else p1(IC<1>{});
    check_tags(ws, f1v, tag, f1t);
    STAMP(7);
    unsigned kb = WS_K + (head * 64 + kq * 16) * 1024;
    const unsigned vo = lane * 16, vv = (WS_V - WS_K) + (lane >> 5) * 256 + r16 * 16;
    u32x16 kv0, kv1, kv2;
    {
        const unsigned c1 = (lane & 27) == 16 ? 0x3F803F80u : 0u;
        asm volatile(A_CST8(112) A_CST8(124) A_L01 A_CST8(136) A_CST8(148) A_L23
                     : "=&{v[112:127]}"(kv0), "=&{v[128:143]}"(kv1), "=&{v[144:159]}"(kv2), [kb] "+s"(kb)
                     : [vo] "v"(vo), [vv] "v"(vv), [cst] "v"((u32x2){c1, c1}), [srd] "s"(sws), [msk] "s"(0x0000FFFF0000FFFFull)
                     : "memory", "scc");
    }
    const unsigned ov = WS_A + (2 * tile + (g4 & 1)) * NKS * 1024 + (g4 >> 1) * 256 + r16 * 16;
    const unsigned ovo = ov + (43 + cg) * 1024 + e * 512;
    st_ws(ws, ovo, 0, (u32x4){~0u, ~0u, ~0u, ~0u});
    const int m3 = w & 1, kk = (w >> 1) & 1;
    unsigned vw = (8 * w + (lane >> 3)) * 1024 + cb * 128 + (((lane & 7) ^ ((g4 & 1) << 1) ^ (m3 << 2)) << 4);
    unsigned wb3 = lds0 + (32 * kk + 8 * g4 + (r16 >> 2)) * 128 + (((r16 >> 3) | ((g4 & 1) << 1)) << 5) + 8 * (lane & 3);
    opaque(vw); opaque(wb3);
    float g = __builtin_amdgcn_rcpf(1.f + __builtin_amdgcn_exp2f(-((g4 & 1) ? z1 : z0)));
    float io[2] = {1.f + __builtin_amdgcn_exp2f(fminf(z0, 126.f)), 1.f + __builtin_amdgcn_exp2f(fminf(z1, 126.f))};
    asm volatile("" : "+v"(g), "+v"(io[0]), "+v"(io[1]));
    if (nh == 0) LGKM_BAR();
    {
        constexpr float C0 = -2.f * 0.7978845608028654f * LOG2E, C1 = C0 * 0.044715f;
        const unsigned hv0 = ov + (ustart + 3 * nh) * 512;
#pragma unroll
        for (int u = 0; u < 3; ++u) {
            f32x4 hv[2];
#pragma unroll
            for (int m = 0; m < 2; ++m)
#pragma unroll
                for (int i = 0; i < 4; i += 2) {
                    const f32x2 t = {af[2 * u][m][i], af[2 * u][m][i + 1]}, up = {af[2 * u + 1][m][i], af[2 * u + 1][m][i + 1]};
                    const f32x2 a = t * __builtin_elementwise_fma(t * t, (f32x2){C1, C1}, (f32x2){C0, C0});
                    const f32x2 ex = {__builtin_amdgcn_exp2f(a[0]), __builtin_amdgcn_exp2f(a[1])};
                    const f32x2 d = __builtin_elementwise_fma(ex, (f32x2){io[m], io[m]}, (f32x2){io[m], io[m]});
                    const f32x2 hh = (t * up) * (f32x2){__builtin_amdgcn_rcpf(d[0]), __builtin_amdgcn_rcpf(d[1])};
                    hv[m][i] = hh[0]; hv[m][i + 1] = hh[1];
                }
            st_ws(ws, hv0, u < 2 ? u * 512 : (3 * nh + 2 < nunit ? 1024 : 0x7f000000u), join16(hv[0], hv[1]));
        }
    }
    STAMP(8);
    const u32x4 sd = make_srd(pwd, FF * DM * 2), so = make_srd(pwo, DM * DM * 2);
    const unsigned ab = WS_A + ((rt * 4 + m3 * 2) * NKS + kk) * 1024;
    f32x16 o3;
    u32x16 h0, h1, h2, h3, h4;
    {
        const unsigned rd = lds0 + LDS_RED + e * 12 * 2304;
        unsigned wl = lds0 + w * 1024, wo = 0, hb = ab + (nh ? 0 : 22 * 1024), cap = SPIN_CAP;
        u64 tmp;
        asm volatile(
            "v_mov_b32 v160, 0xdddddddd\n\t"
            A_Z(88) A_Z(0) A_Z(16) A_Z(32) A_Z(48)
            A_W(10)
            "v_mfma_f32_32x32x16_bf16 v[64:79], v[112:115], v[96:99], 0\n\t"
            "v_mov_b32 v8, 0\n\tv_mov_b32 v24, 0\n\tv_mov_b32 v40, 0\n\tv_mov_b32 v56, 0\n\t"
            "s_nop 7\n\t"
            ".p2align 3\n\t"
            A_S(0) A_KEYT(112, 116, 120, 120, 124, "", "", "", "", "") A_T1("", "", "", A_W(4))
            A_S(1) A_T2(A_L01, A_D, A_D) A_T3(A_D, A_D, "", A_W(4) A_POST)
            A_S(2) A_P(2) A_T0(A_L23, A_D, A_D, "") A_T1(A_D, A_D, A_D, A_W(5))
            A_S(3) A_T2(A_L01, A_D, A_D) A_T3(A_D, A_D, A_WARM, A_W(6))
            A_S(4) A_T0(A_POLL A_L23, "", "", A_W(6) A_CHECK) A_T1(A_H(164), A_H(172), A_H(180), A_W(6))
            A_S(5) A_T2(A_L01, A_H(188), A_H(196)) A_T3(A_H(204), A_H(212), A_H(220), A_W(10))
            A_S(6) A_P(1) A_T0(A_L23, A_H(228), A_H(236), "") A_T1(A_CODE, "", "", A_W(5))
            A_S(7) A_LAST
            A_RA(r3)
            "v_smfmac_f32_32x32x32_bf16 v[48:63], v[156:159], v[88:95], v160\n\t"
            "s_waitcnt vmcnt(0) lgkmcnt(0)\n\t"
            "s_barrier\n\t"
            A_RD(0, 0) A_RD(16, 2304) A_RD(32, 4608)
            "s_waitcnt lgkmcnt(3)\n\t"
            A_ADD(0, 16)
            "s_waitcnt lgkmcnt(0)\n\t"
            "s_barrier\n\t"
            A_ADD(48, 32) A_ADD(48, 0)
            : "=&{v[48:63]}"(o3), "+{v[112:127]}"(kv0), "+{v[128:143]}"(kv1), "+{v[144:159]}"(kv2),
              "=&{v[164:179]}"(h0), "=&{v[180:195]}"(h1), "=&{v[196:211]}"(h2), "=&{v[212:227]}"(h3), "=&{v[228:243]}"(h4),
              [kb] "+s"(kb), [wl] "+s"(wl), [wo] "+s"(wo), [hb] "+s"(hb), [cap] "+s"(cap), [tmp] "=&s"(tmp) A_SOUT
            : "{v[96:111]}"(qf), [vo] "v"(vo), [vv] "v"(vv), [vw] "v"(vw),
              [srd] "s"(sws), [sd] "s"(sd), [so] "s"(so), [msk] "s"(0x0000FFFF0000FFFFull), [tg0] "s"(tag.lo), [tg1] "s"(tag.hi),
              [fh] "s"(WS_FH + (tile * 32 + 2 * cg + nh) * 128), [fp] "s"(WS_FH + (rt * 2 + m3) * 4096),
              [w1] "s"((832 + ((rt & 3) * 8 + w) * 17) * 1024 + cb * 128), [w2] "s"(((rt & 3) * 8 + w) * 16 * 1024 + cb * 128),
              [r0] "s"(rd + ((kq + 1) & 3) * 3 * 2304), [r1] "s"(rd + (((kq + 2) & 3) * 3 + 1) * 2304), [r2] "s"(rd + (((kq + 3) & 3) * 3 + 2) * 2304),
              [r3] "s"(rd + kq * 3 * 2304), [wc] "s"(w * 2048), [hz] "s"(w == 2 || w == 3 ? 0x70000000u : 0u)
            : "memory", "scc", "vcc", "s80", "s81", "s82", "s83", "v0", "v1", "v2", "v3", "v4", "v5", "v6", "v7", "v8", "v9", "v10", "v11", "v12", "v13", "v14", "v15",
              "v16", "v17", "v18", "v19", "v20", "v21", "v22", "v23", "v24", "v25", "v26", "v27", "v28", "v29", "v30", "v31",
              "v32", "v33", "v34", "v35", "v36", "v37", "v38", "v39", "v40", "v41", "v42", "v43", "v44", "v45", "v46", "v47",
              "v64", "v65", "v66", "v67", "v68", "v69", "v70", "v71", "v72", "v73", "v74", "v75", "v76", "v77", "v78", "v79",
              "v80", "v81", "v82", "v83", "v84", "v85", "v86", "v87", "v88", "v89", "v90", "v91", "v92", "v93", "v94", "v95",
              "v160", "v161", "v162", "v163");
    }
    STAMP(9);
    asm volatile("" : "+v"(lane));
    r16 = lane & 15; g4 = lane >> 4;
    {
        f32x8 oo = __builtin_shufflevector(o3, o3, 0, 1, 2, 3, 4, 5, 6, 7);
        float l = o3[8];
        float am = 0.f;
#pragma unroll
        for (int j = 0; j < 8; ++j) am = fmaxf(am, fabsf(oo[j]));
        const bool ok = l >= 0x1p-40f && l <= 0x1p40f && am <= 0x1p120f;
        auto finish = [&]() __attribute__((always_inline)) {
            const float sc = g * __builtin_amdgcn_rcpf(l);
            u32x4 p;
#pragma unroll
            for (int j = 0; j < 4; ++j) p[j] = min(pk2(oo[2 * j] * sc, oo[2 * j + 1] * sc), 0xFFFFFFFEu);
            st_ws(ws, ovo, 0, p);
        };
        if (__builtin_expect(__any(!ok), 0)) {
            l = attn_safe(ws, __builtin_bit_cast(bf16x8, (u32x4){qf[12], qf[13], qf[14], qf[15]}), oo, WS_K + head * 65536, lane);
            finish();
            asm volatile("; guarded");
        } else finish();
        SCHED();
    }
    STAMP(10);
    bf16x8 a3[11][2];
    {
#pragma unroll
        for (int m = 0; m < 2; ++m) a3[10][m] = __builtin_bit_cast(bf16x8, (u32x4){kv0[4 * m], kv0[4 * m + 1], kv0[4 * m + 2], kv0[4 * m + 3]});
        const u32x16 hh[5] = {h0, h1, h2, h3, h4};
#pragma unroll
        for (int st = 0; st < 10; ++st)
#pragma unroll
            for (int m = 0; m < 2; ++m) {
                const int q = (st & 1) * 8 + m * 4;
                a3[st][m] = __builtin_bit_cast(bf16x8, (u32x4){hh[st >> 1][q], hh[st >> 1][q + 1], hh[st >> 1][q + 2], hh[st >> 1][q + 3]});
            }
    }
    const unsigned vw0 = (vw ^ (m3 << 6)) + (16 * mt - 8 * w) * 1024, vw1 = (vw0 ^ 64) + 8192;
    auto wblock = [&](bool o_rows, int blk, int slot) {
        dma16(o_rows ? so : sd, vw0, blk * 65536, lds0 + slot * HS + mt * 2048);
        dma16(o_rows ? so : sd, vw1, blk * 65536, lds0 + slot * HS + mt * 2048 + 1024);
    };
    f32x4 acc[4][2];
    unsigned da, ra, oa;
    auto addrs = [&](int i) {
        if (i == 0) { da = lds0 + LDS_SUM + ((w >> 1) * 64 + m3 * 32 + r16) * 256 + ((((g4 ^ r16) & 1) * 8 + ((g4 >> 1) ^ (r16 & 7))) << 4); opaque(da); }
        if (i == 1) { ra = lds0 + LDS_SUM + (w * 8 + (lane >> 3)) * 256 + ((((lane >> 3) & 1) * 8 + ((lane ^ (lane >> 3)) & 7)) << 4); opaque(ra); }
        if (i == 2) { oa = (rt * 64 + w * 8 + (lane >> 3)) * 1024 + cb * 128 + (lane & 7) * 16; opaque(oa); }
        SCHED();
    };
    constexpr int PD3 = 4;
    auto stream = [&](auto NBC, auto S0C, auto FSTC, auto& a, auto&& hook) {
        constexpr int NB = NBC.v, S0 = S0C.v;
        s16x4 wlo[8], whi[8];
        unsigned b3[4];
        static_for<-PD3, 4 * NB>([&](auto I) {
            constexpr int n = I.v, nn = n + PD3, slot = (S0 + nn / 4) % 20, nun = nn % 4, st = n / 4, nu = n % 4;
            constexpr bool fetch = nn < 4 * NB, fst = FSTC.v && n < 4;
            hook(I);
            if constexpr (fetch && (nn < 4 || slot % 8 == 0)) { b3[nun] = (wb3 ^ (nun * 32)) + (slot & ~7) * HS; opaque(b3[nun]); }
            SCHED();
            if constexpr (n >= 0) mma<fst>(acc[nu][0], cat8(wlo[n & 7], whi[n & 7]), a[st][0]);
            SCHED();
            if constexpr (fetch) wlo[nn & 7] = TRREAD(b3[nun] + (slot & 7) * HS);
            SCHED();
            if constexpr (n >= 0) mma<fst>(acc[nu][1], cat8(wlo[n & 7], whi[n & 7]), a[st][1]);
            SCHED();
            if constexpr (fetch) whi[nn & 7] = TRREAD(b3[nun] + (slot & 7) * HS + 512);
            SCHED();
        });
    };
    auto dump = [&]() {
        mfma_done();
#pragma unroll
        for (int nu = 0; nu < 4; ++nu)
#pragma unroll
            for (int m = 0; m < 2; ++m)
                *(LAS f32x4*)(size_t)((da ^ (nu * 32)) + m * 4096) = acc[nu][m];
    };
    if (nh == 1) {
        __builtin_amdgcn_s_setprio(1);
        stream(IC<11>{}, IC<0>{}, IC<1>{}, a3, [&](auto I) {
            if constexpr (I.v == 20) { BAR(); }
        });
        BAR();
        STAMP(11);
        u32x16 oq[4];
        bf16x8 ao[8][2];
        stream(IC<8>{}, IC<2>{}, IC<0>{}, ao, [&](auto I) {
            if constexpr (I.v == -4) wblock(true, 3, 5);
            if constexpr (I.v < 0) { wblock(true, 8 + I.v, 10 + I.v); SCHED(); addrs(I.v + 4); }
            if constexpr (I.v == 0) o_fetch(oq, sws, lane * 16, ab + 43 * 1024);
            if constexpr (I.v >= 0 && I.v % 8 == 0) {
                constexpr int q = I.v / 8;
                o_check<q>(oq, sws, lane * 16, ab + 43 * 1024);
#pragma unroll
                for (int i = 0; i < 4; ++i)
                    ao[2 * q + (i >> 1)][i & 1] = __builtin_bit_cast(bf16x8, (u32x4){oq[q][4 * i], oq[q][4 * i + 1], oq[q][4 * i + 2], oq[q][4 * i + 3]});
            }
            if constexpr (I.v == 0) { BAR(); STAMP(12); }
            if constexpr (I.v >= 0 && I.v < 8) o_more<I.v & 7>(oq, sws, lane * 16, ab + 43 * 1024);
        });
        STAMP(13);
    } else {
        stream(IC<11>{}, IC<11>{}, IC<1>{}, a3, [&](auto I) {
            if constexpr (I.v < 0) {
                wblock(false, 21 + 2 * I.v, 21 + 2 * I.v);
                if constexpr (I.v < -1) { SCHED(); addrs(I.v + 4); wblock(false, 22 + 2 * I.v, 22 + 2 * I.v); }
            }
            if constexpr (I.v == 0) {
                BAR();
                wblock(true, 0, 2); wblock(false, 20, 0); wblock(false, 21, 1); wblock(true, 1, 3); wblock(true, 2, 4);
                vm_bar<8>();
                STAMP(11);
            }
            if constexpr (I.v == 32) { vm_bar<0>(); STAMP(12); }
        });
        STAMP(13);
    }
    dump();
    LGKM_BAR();
    STAMP(14);
    f32x4 ps[4][2];
#pragma unroll
    for (int q = 0; q < 4; ++q)
#pragma unroll
        for (int i = 0; i < 2; ++i) ps[q][i] = *(const LAS f32x4*)(size_t)((ra ^ (i * 128)) + q * 16384);
    SCHED();
    const f32x4 lo = (ps[0][0] + ps[1][0]) + (ps[2][0] + ps[3][0]), hi = (ps[0][1] + ps[1][1]) + (ps[2][1] + ps[3][1]);
    st_ws(make_rsrc(pout, SEQ * DM * 2), oa, 0, (u32x4){pk2(lo[0], lo[1]), pk2(lo[2], lo[3]), pk2(hi[0], hi[1]), pk2(hi[2], hi[3])});
    asm volatile(".Lgm_code_hi:");
    if (b == 0 && w == 0) post_tag(ws, WS_EP, tag, lane);
}

extern "C" void kernel_launch(void* const* d_in, const int* in_sizes, int n_in, void* d_out, int out_size, void* d_ws, size_t ws_size,
                              hipStream_t stream) {
    (void)hipFuncSetAttribute((const void*)gm_kernel, hipFuncAttributeMaxDynamicSharedMemorySize, LDS_BYTES);
    gm_kernel<<<256, 512, LDS_BYTES, stream>>>(d_in[0], d_in[1], d_in[4], d_in[5], d_in[7], d_ws, d_in[6], d_in[2], d_out);
}
```
